```python
import torch

def _softmax(x, axis=-1):
    return torch.softmax(x, dim=axis)
def _gelu(x):
    return torch.nn.functional.gelu(x, approximate="tanh")
def _silu(x):
    return torch.nn.functional.silu(x)
def _sigmoid(x):
    return torch.sigmoid(x)
def _layernorm(x, axis=-1, eps=1e-5):
    m = x.mean(dim=axis, keepdim=True)
    v = x.var(dim=axis, keepdim=True, unbiased=False)
    return (x - m) / torch.sqrt(v + eps)
def _scatter_add(tgt, idx, src, axis=0):
    return tgt.clone().index_add_(axis, idx, src)
def _segment_sum(src, seg, n):
    out = torch.zeros((n,) + src.shape[1:], dtype=src.dtype)
    return out.index_add_(0, seg, src)
def _pad(x, pw):
    flat = tuple(v for pair in reversed(pw) for v in pair)
    return torch.nn.functional.pad(x, flat)

kernel_name = 'recurrent_scan__ssm_conv_s5__swiglu_ffn__prompt'

def setup_inputs():

    _w = (lambda a: (lambda t: t.to(torch.bfloat16) if t.is_floating_point() else t)(torch.as_tensor(a)))
    def _A_setup():
        B, S, D = 1, 2048, 448
        P = D
        Lambda_re = -0.5 * (lambda _x: torch.exp(torch.as_tensor(_x)))((lambda *s: torch.randn(*s, dtype=torch.bfloat16) * 0.2)(P))
        Lambda_im = (lambda *s: torch.randn(*s, dtype=torch.bfloat16) * 0.4)(P) * 1.0
        B_re = (lambda *s: torch.randn(*s, dtype=torch.bfloat16) * 0.2)(P, D) * (D ** -0.5)
        B_im = (lambda *s: torch.randn(*s, dtype=torch.bfloat16) * 0.2)(P, D) * (D ** -0.5)
        C_re = (lambda *s: torch.randn(*s, dtype=torch.bfloat16) * 0.2)(D, P) * (P ** -0.5)
        C_im = (lambda *s: torch.randn(*s, dtype=torch.bfloat16) * 0.2)(D, P) * (P ** -0.5)
        log_dt = (lambda *s: torch.randn(*s, dtype=torch.bfloat16) * 0.2)(P) * 0.3 - 2.0
        Dskip = (lambda *s: torch.randn(*s, dtype=torch.bfloat16) * 0.05)(D)
        return {
            "x": _w((lambda *s: torch.randn(*s, dtype=torch.bfloat16) * 0.05)(B, S, D)),
            "Lambda_re": _w(Lambda_re),
            "Lambda_im": _w(Lambda_im),
            "B_re": _w(B_re),
            "B_im": _w(B_im),
            "C_re": _w(C_re),
            "C_im": _w(C_im),
            "log_dt": _w(log_dt),
            "Dskip": _w(Dskip),
        }
    _A_in = _A_setup()

    def _B_setup():
        B, S, D, F = 1, 2048, 448, 1792
        return {
            "x": _w((lambda *s: torch.randn(*s, dtype=torch.bfloat16) * 0.05)(B, S, D)),
            "w_gate": _w((lambda *s: torch.randn(*s, dtype=torch.bfloat16) * 0.05)(D, F)),
            "w_up": _w((lambda *s: torch.randn(*s, dtype=torch.bfloat16) * 0.05)(D, F)),
            "w_down": _w((lambda *s: torch.randn(*s, dtype=torch.bfloat16) * 0.05)(F, D)),
        }
    _B_in = _B_setup()

    return {
        "A_x": _A_in["x"],
        "A_Lambda_re": _A_in["Lambda_re"],
        "A_Lambda_im": _A_in["Lambda_im"],
        "A_B_re": _A_in["B_re"],
        "A_B_im": _A_in["B_im"],
        "A_C_re": _A_in["C_re"],
        "A_C_im": _A_in["C_im"],
        "A_log_dt": _A_in["log_dt"],
        "A_Dskip": _A_in["Dskip"],
        "B_x": _B_in["x"],
        "B_w_gate": _B_in["w_gate"],
        "B_w_up": _B_in["w_up"],
        "B_w_down": _B_in["w_down"],
    }

def reference(A_x, A_Lambda_re, A_Lambda_im, A_B_re, A_B_im, A_C_re, A_C_im, A_log_dt, A_Dskip, B_x, B_w_gate, B_w_up, B_w_down):
    def _A_ref(x, Lambda_re, Lambda_im, B_re, B_im, C_re, C_im, log_dt, Dskip):
        Bsz, L, H = x.shape
        Lam = Lambda_re + 1j * Lambda_im
        Bmat = B_re + 1j * B_im
        Cmat = C_re + 1j * C_im
        dt = torch.exp(log_dt)
        Lam_bar = torch.exp(dt * Lam)
        B_bar = ((Lam_bar - 1.0) / Lam)[:, None] * Bmat
        Bu = torch.einsum("bld,pd->blp", x + 0j, B_bar)
        h = torch.zeros_like(Bu[:, 0, :])
        hs = []
        for t in range(L):
            h = Lam_bar[None, :] * h + Bu[:, t, :]
            hs.append(h)
        hseq = torch.stack(hs, 1)
        y = 2.0 * torch.einsum("blp,dp->bld", hseq, Cmat).real + x * Dskip[None, None, :]
        return y

    def _B_ref(x, w_gate, w_up, w_down):
        g = _silu(x @ w_gate)
        u = x @ w_up
        return (g * u) @ w_down

    x = A_x
    xn = _layernorm(x, axis=-1)
    s = _A_ref(xn, A_Lambda_re, A_Lambda_im, A_B_re, A_B_im, A_C_re, A_C_im, A_log_dt, A_Dskip)
    g = _silu(_B_ref(xn, B_w_gate, B_w_up, B_w_down))
    return x + s * g
```

```cpp
#include <hip/hip_runtime.h>
#include <hip/hip_bf16.h>
#include <stdint.h>
#include <utility>

typedef __attribute__((ext_vector_type(8))) __bf16 bf16x8;
typedef __attribute__((ext_vector_type(4))) __bf16 bf16x4;
typedef __attribute__((ext_vector_type(2))) __bf16 bf16x2;
typedef __attribute__((ext_vector_type(16))) float f32x16;
typedef __attribute__((ext_vector_type(4))) float f32x4;
typedef __attribute__((ext_vector_type(2))) float f32x2;
typedef __attribute__((ext_vector_type(4))) unsigned u32x4;
typedef __attribute__((ext_vector_type(2))) unsigned u32x2;
typedef unsigned long long u64;
#define LDS3 __attribute__((address_space(3)))
#define GLB1 __attribute__((address_space(1)))
#define DEVI __device__ __forceinline__

constexpr int DM = 448, FF = 1792;
constexpr int KS_ALL = 168;
constexpr size_t OFF_AGG = 0;
constexpr size_t AGG_BYTES = 16 * 14 * 64 * 8;
constexpr size_t OFF_A = 131072;
constexpr size_t OFF_B = OFF_A + (size_t)64 * KS_ALL * 1024;
constexpr size_t OFF_ST = OFF_B + (size_t)14 * KS_ALL * 1024;

template <int... Is, class Fn>
DEVI void static_for_impl(std::integer_sequence<int, Is...>, Fn&& f) {
  (f(std::integral_constant<int, Is>{}), ...);
}
template <int N, class Fn>
DEVI void static_for(Fn&& f) { static_for_impl(std::make_integer_sequence<int, N>{}, f); }

DEVI void glds16(unsigned voff, const void* sbase, unsigned ldsdst) {
  unsigned keep;
  asm volatile("s_mov_b32 %0, m0\n\ts_mov_b32 m0, %3\n\ts_nop 0\n\tglobal_load_lds_dwordx4 %1, %2\n\ts_mov_b32 m0, %0"
               : "=&s"(keep) : "v"(voff), "s"(sbase), "s"(ldsdst) : "memory");
}
template <int N> DEVI void wait_vm_lgkm0() { asm volatile("s_waitcnt vmcnt(%0) lgkmcnt(0)" ::"n"(N) : "memory"); }
DEVI float bf2f(unsigned short v) { return __uint_as_float((unsigned)v << 16); }
DEVI unsigned pack2(float a, float b) {
  f32x2 p = {a, b};
  return __builtin_bit_cast(unsigned, __builtin_convertvector(p, bf16x2));
}
DEVI float dot2(unsigned a, unsigned b, float c) {
  return __builtin_amdgcn_fdot2_f32_bf16(__builtin_bit_cast(bf16x2, a), __builtin_bit_cast(bf16x2, b), c, false);
}
DEVI float sum8(bf16x8 f, float c) {
  u32x4 u = __builtin_bit_cast(u32x4, f);
#pragma unroll
  for (int i = 0; i < 4; ++i) c = dot2(u[i], 0x3f803f80u, c);
  return c;
}
DEVI float sq8(bf16x8 f, float c) {
  u32x4 u = __builtin_bit_cast(u32x4, f);
#pragma unroll
  for (int i = 0; i < 4; ++i) c = dot2(u[i], u[i], c);
  return c;
}
DEVI bf16x8 lds_b128(unsigned addr) { return *(LDS3 bf16x8*)(uintptr_t)addr; }
DEVI bf16x8 lds_tr(unsigned a0, unsigned a1) {
  bf16x4 a = __builtin_amdgcn_ds_read_tr16_b64_v4bf16((LDS3 bf16x4*)(uintptr_t)a0);
  bf16x4 b = __builtin_amdgcn_ds_read_tr16_b64_v4bf16((LDS3 bf16x4*)(uintptr_t)a1);
  return __builtin_shufflevector(a, b, 0, 1, 2, 3, 4, 5, 6, 7);
}
DEVI f32x16 mfma(bf16x8 a, bf16x8 b, f32x16 c) { return __builtin_amdgcn_mfma_f32_32x32x16_bf16(a, b, c, 0, 0, 0); }
DEVI float silu(float g) { return g * __builtin_amdgcn_rcpf(1.0f + __expf(-g)); }
DEVI void split3(float v, unsigned& h, unsigned& m, unsigned& l) {
  h = pack2(v, 0.f) & 0xffffu;
  float r1 = v - __uint_as_float(h << 16);
  m = pack2(r1, 0.f) & 0xffffu;
  float r2 = r1 - __uint_as_float(m << 16);
  l = pack2(r2, 0.f) & 0xffffu;
}
DEVI bf16x8 corr_x(float negmean) {
  unsigned h, m, l; split3(negmean, h, m, l);
  u32x4 u = {h | (m << 16), h | (m << 16), l | (h << 16), 0u};
  return __builtin_bit_cast(bf16x8, u);
}
DEVI bf16x8 corr_w(float cs) {
  unsigned h, m, l; split3(cs, h, m, l);
  u32x4 u = {h | (h << 16), m | (m << 16), h | (l << 16), 0u};
  return __builtin_bit_cast(bf16x8, u);
}
DEVI int tau(int r) { return 16 * ((r >> 2) & 1) + (r & 3) + 4 * (r >> 3); }

constexpr int SLOT = 32768;
constexpr int L_CS = 4 * SLOT;
constexpr int L_ST = L_CS + 9216;
constexpr int L_RS = L_ST + 4096;
constexpr int L_SEG = L_RS + 512;
constexpr int L_TOT = L_SEG + 2048;
constexpr int L_HT = 2 * SLOT;

struct K1Args {
  const unsigned short *x, *lre, *lim, *bre, *bim, *cre, *cim, *ldt, *wg, *wu, *wd;
  char* ws;
};

template <int G, int NH> constexpr int gcnt() { return G > 13 ? 0 : (G < 7 ? 4 : (NH ? 2 : 3)); }

template <int ROLE>
DEVI void k1_body(const K1Args& a, const unsigned lb, const int c, const int j, const int w, const int lane) {
  constexpr int NH = (ROLE == 1) ? 1 : 0;
  const int mq = w & 3;
  const int r = lane & 31, h = lane >> 5;
  const bool prep = j >= 14;
  const int pq = c * 2 + (j - 14);
  char* const wsA = a.ws + OFF_A;
  char* const wsB = a.ws + OFF_B;
  const int rb = 4 * c + mq;

  unsigned p_re = 0, p_im = 0, p_dt = 0;
  if constexpr (ROLE == 0) {
    const int ch = 32 * j + r;
    asm volatile("global_load_ushort %0, %1, off" : "=v"(p_re) : "v"(a.lre + ch) : "memory");
    asm volatile("global_load_ushort %0, %1, off" : "=v"(p_im) : "v"(a.lim + ch) : "memory");
    asm volatile("global_load_ushort %0, %1, off" : "=v"(p_dt) : "v"(a.ldt + ch) : "memory");
  }

  unsigned vo1[4], vo2[3];
  const char* b1; const char* b2;
  int st1, st1b = 0;
  unsigned ld1, ld2;
  if (NH == 0) {
#pragma unroll
    for (int n = 0; n < 4; ++n) {
      int rho = 8 * n + (lane >> 3);
      vo1[n] = tau(rho) * 896 + (((lane & 7) ^ ((rho >> 1) & 7)) << 4);
    }
    b1 = (const char*)a.x + (size_t)(128 * c + 32 * mq) * 896; st1 = 128; ld1 = mq * 4096;
  } else if (mq < 2) {
    if (!prep) {
#pragma unroll
      for (int n = 0; n < 4; ++n) {
        int rho = 8 * n + (lane >> 3);
        vo1[n] = rho * 896 + (((lane & 7) ^ ((rho >> 1) & 7)) << 4);
      }
      b1 = (const char*)(mq ? a.bim : a.bre) + (size_t)(32 * j) * 896; st1 = 128;
    } else {
#pragma unroll
      for (int n = 0; n < 4; ++n) {
        int pc = min(4 * mq + n, 6);
        vo1[n] = (lane >> 2) * 896 + pc * 64 + ((lane & 3) << 4);
      }
      b1 = (const char*)a.wd; st1 = 0; st1b = 1;
    }
    ld1 = 16384 + mq * 4096;
  } else {
    const int isup = mq & 1;
#pragma unroll
    for (int n = 0; n < 4; ++n) vo1[n] = (16 * n + (lane >> 2)) * 3584 + (((lane & 3) ^ (2 * isup)) << 4);
    b1 = (const char*)(isup ? a.wu : a.wg) + 224 * j; st1 = 64 * 3584; ld1 = 24576 + isup * 4096;
  }
  {
    const int isup = mq >> 1;
    const int n0 = NH ? 6 + 2 * (mq & 1) : 3 * (mq & 1);
#pragma unroll
    for (int n = 0; n < 3; ++n) {
      int b = (n0 + n) * 64 + lane;
      int row = b / 10, cc = b - row * 10;
      vo2[n] = row * 3584 + cc * 16;
    }
    b2 = (const char*)(isup ? a.wu : a.wg) + 224 * j + 64;
    ld2 = isup * 10368 + n0 * 1024;
  }
  auto stage = [&](auto GI) {
    constexpr int g = GI;
    if constexpr (g < 7) {
      const char* b = b1 + g * st1;
      if (NH == 1 && st1b) { int v = 7 * pq + g; b = b1 + (v >> 1) * (16 * 896) + (v & 1) * 448; }
#pragma unroll
      for (int n = 0; n < 4; ++n) glds16(vo1[n], b, lb + (g & 3) * SLOT + ld1 + n * 1024);
    } else if constexpr (g < 14) {
      const char* b = b2 + (g - 7) * (64 * 3584);
#pragma unroll
      for (int n = 0; n < (NH ? 2 : 3); ++n) glds16(vo2[n], b, lb + (g & 3) * SLOT + ld2 + n * 1024);
    }
  };
  static_for<4>(stage);

  unsigned boff[4];
#pragma unroll
  for (int ks = 0; ks < 4; ++ks) boff[ks] = lb + r * 128 + (((2 * ks + h) ^ ((r >> 1) & 7)) << 4);
  const int isupl = (lane >> 4) & 1, q4 = (lane >> 2) & 3, p4 = lane & 3;
  const unsigned w1a = lb + 24576 + isupl * 4096 + (8 * h + q4) * 64 + p4 * 8;
  const unsigned w2a = lb + isupl * 10368 + (8 * h + q4) * 160 + p4 * 8 + (NH ? 64 : 0);
  const unsigned wpa = lb + 16384 + (4 * h + q4) * 64 + isupl * 32 + p4 * 8;

  bf16x8 xf[28];
  bf16x8 fr[2][3];
  f32x16 acc1[2] = {}, acc2[3] = {};
  float cs1[2] = {0.f, 0.f}, cs2[3] = {0.f, 0.f, 0.f}, cs3[3] = {0.f, 0.f, 0.f};
  float s1 = 0.f, s2 = 0.f, rstd = 0.f;
  bf16x8 cx;
  float lr = 0.f, li = 0.f, l16r = 0.f, l16i = 0.f, clr = 0.f, cli = 0.f;
  u64 gr[15];
  u32x4 cpr[2][2], cpi[2][2];
  GLB1 u64* const aggp = (GLB1 u64*)(a.ws + OFF_AGG);

  auto load_frags = [&](auto SI) {
    constexpr int s = SI, g = s >> 2, ks = s & 3, sl = (g & 3) * SLOT, pb = s & 1;
    if constexpr (g < 7) {
      xf[s] = lds_b128(boff[ks] + mq * 4096 + sl);
      if constexpr (ROLE == 0) {
        fr[pb][0] = lds_b128(boff[ks] + sl + 16384);
        fr[pb][1] = lds_b128(boff[ks] + sl + 20480);
      } else if constexpr (ROLE == 1) {
#pragma unroll
        for (int t = 0; t < 2; ++t) {
          unsigned ad = w1a + ((t ^ isupl) * 32) + sl + ks * 1024;
          fr[pb][t] = lds_tr(ad, ad + 256);
        }
      } else {
        if constexpr (ks < 2) {
          unsigned ad = wpa + sl + (mq + 4 * ks) * 1024;
          fr[pb][0] = lds_tr(ad, ad + 512);
        }
      }
    } else {
#pragma unroll
      for (int t = 0; t < (NH ? 3 : 2); ++t) {
        unsigned ad = w2a + t * 32 + sl + ks * 2560;
        fr[pb][t] = lds_tr(ad, ad + 640);
      }
    }
  };
  auto ffn_epi = [&](const f32x16& ac, int T) {
    u32x4 o;
#pragma unroll
    for (int i = 0; i < 4; ++i) {
      float v0 = silu(ac[2 * i] * rstd) * (ac[8 + 2 * i] * rstd);
      float v1 = silu(ac[2 * i + 1] * rstd) * (ac[9 + 2 * i] * rstd);
      o[i] = pack2(v0, v1);
    }
    *(u32x4*)(wsA + (size_t)(rb * KS_ALL + 56 + 7 * j + T) * 1024 + lane * 16) = o;
  };
  auto cs_read = [&](int tile) {
    f32x4 v = *(LDS3 f32x4*)(uintptr_t)(lb + L_CS + tile * 1024 + lane * 16);
    return (v[0] + v[1]) + (v[2] + v[3]);
  };
  auto cs_write = [&](int tile, float v) { *(LDS3 float*)(uintptr_t)(lb + L_CS + tile * 1024 + lane * 16 + mq * 4) = v; };

  asm volatile("s_waitcnt vmcnt(12)" : "+v"(p_re), "+v"(p_im), "+v"(p_dt)::"memory");
  __builtin_amdgcn_s_barrier();
  load_frags(std::integral_constant<int, 0>{});

  static_for<56>([&](auto SI) {
    constexpr int s = SI, g = s >> 2, ks = s & 3, pb = s & 1;
    if constexpr (ks == 3) {
      wait_vm_lgkm0<gcnt<g + 2, NH>() + gcnt<g + 3, NH>()>();
      __builtin_amdgcn_s_barrier();
      stage(std::integral_constant<int, g + 4>{});
    }
    if constexpr (s + 1 < 56) load_frags(std::integral_constant<int, s + 1>{});
    __builtin_amdgcn_sched_barrier(0);
    if constexpr (g < 7) {
      if constexpr (ROLE == 0) {
        acc1[0] = mfma(xf[s], fr[pb][0], acc1[0]);
        acc1[1] = mfma(xf[s], fr[pb][1], acc1[1]);
      } else if constexpr (ROLE == 1) {
        acc1[0] = mfma(fr[pb][0], xf[s], acc1[0]);
        acc1[1] = mfma(fr[pb][1], xf[s], acc1[1]);
      } else if constexpr (ks < 2) {
        const int pc = mq + 4 * ks;
        if (pc < 7) {
          int v = 7 * pq + g;
          *(bf16x8*)(wsB + (size_t)(((v & 1) * 7 + pc) * KS_ALL + 56 + (v >> 1)) * 1024 + lane * 16) = fr[pb][0];
        }
      }
      if constexpr ((ks & 1) == NH) { s1 = sum8(xf[s], s1); s2 = sq8(xf[s], s2); }
      if constexpr (ROLE != 2) {
        if (mq == ks) { cs1[0] = sum8(fr[pb][0], cs1[0]); cs1[1] = sum8(fr[pb][1], cs1[1]); }
      }
    } else {
      constexpr int NT = NH ? 3 : 2;
#pragma unroll
      for (int t = 0; t < NT; ++t) acc2[t] = mfma(fr[pb][t], xf[s - 28], acc2[t]);
      if constexpr (g < 13) {
        if (mq == ks) {
#pragma unroll
          for (int t = 0; t < NT; ++t) cs2[t] = sum8(fr[pb][t], cs2[t]);
        }
      } else {
#pragma unroll
        for (int t = 0; t < NT; ++t) cs3[t] = sum8(fr[pb][t], cs3[t]);
      }
    }
    if constexpr (s == 1 && ROLE == 0) {
      float dt = expf(bf2f(p_dt)), are = bf2f(p_re), aim = bf2f(p_im);
      float e = expf(dt * are), sn, cn;
      sincosf(dt * aim, &sn, &cn);
      lr = e * cn; li = e * sn;
      float inv = 1.0f / (are * are + aim * aim);
      float nr = lr - 1.0f, ni = li;
      clr = (nr * are + ni * aim) * inv;
      cli = (ni * are - nr * aim) * inv;
      l16r = lr; l16i = li;
#pragma unroll
      for (int i = 0; i < 4; ++i) { float t = l16r * l16r - l16i * l16i; l16i = 2.f * l16r * l16i; l16r = t; }
    }
    if constexpr (s == 27) {
      f32x2 v = {s1, s2};
      *(LDS3 f32x2*)(uintptr_t)(lb + L_ST + (mq * 32 + r) * 32 + NH * 16 + h * 8) = v;
      if constexpr (ROLE != 2) { cs_write(2 * NH, cs1[0]); cs_write(2 * NH + 1, cs1[1]); }
    }
    if constexpr (s == 31) {
      f32x4 u0 = *(LDS3 f32x4*)(uintptr_t)(lb + L_ST + (mq * 32 + r) * 32);
      f32x4 u1 = *(LDS3 f32x4*)(uintptr_t)(lb + L_ST + (mq * 32 + r) * 32 + 16);
      float mean = ((u0[0] + u0[2]) + (u1[0] + u1[2])) * (1.0f / DM);
      float ex2 = ((u0[1] + u0[3]) + (u1[1] + u1[3])) * (1.0f / DM);
      float var = fmaxf(ex2 - mean * mean, 0.f);
      rstd = 1.0f / sqrtf(var + 1e-5f);
      cx = corr_x(-mean);
      if constexpr (ROLE == 1) {
        if (j == 0 && h == 0) {
          f32x2 v = {mean, rstd};
          *(f32x2*)(a.ws + OFF_ST + (size_t)(32 * rb + tau(r)) * 8) = v;
        }
        acc1[0] = mfma(corr_w(cs_read(2)), cx, acc1[0]);
        acc1[1] = mfma(corr_w(cs_read(3)), cx, acc1[1]);
        ffn_epi(acc1[0], 0);
        ffn_epi(acc1[1], 1);
      } else if constexpr (ROLE == 0) {
        *(LDS3 float*)(uintptr_t)(lb + L_RS + mq * 128 + tau(r) * 4) = rstd;
        asm volatile("" ::: "memory");
        acc1[0] = mfma(cx, corr_w(cs_read(0)), acc1[0]);
        acc1[1] = mfma(cx, corr_w(cs_read(1)), acc1[1]);
        float er = 0.f, ei = 0.f;
#pragma unroll
        for (int i4 = 0; i4 < 4; ++i4) {
          f32x4 rs = *(LDS3 f32x4*)(uintptr_t)(lb + L_RS + mq * 128 + h * 64 + i4 * 16);
#pragma unroll
          for (int i = 0; i < 4; ++i) {
            float ur = acc1[0][4 * i4 + i] * rs[i], ui = acc1[1][4 * i4 + i] * rs[i];
            float vr = clr * ur - cli * ui, vi = clr * ui + cli * ur;
            acc1[0][4 * i4 + i] = vr; acc1[1][4 * i4 + i] = vi;
            float t = lr * er - li * ei + vr;
            ei = lr * ei + li * er + vi; er = t;
          }
        }
        f32x2 v = {er, ei};
        *(LDS3 f32x2*)(uintptr_t)(lb + L_SEG + ((2 * mq + h) * 32 + r) * 8) = v;
      } else {
#pragma unroll
        for (int u = 0; u < 2; ++u) {
          int un = min(pq * 4 + mq + 128 * u, 195);
          int cb = un / 14, jc = un - cb * 14;
          size_t o = (size_t)(32 * cb + r) * DM + 32 * jc + 16 * h;
          cpr[u][0] = *(const u32x4*)(a.cre + o); cpr[u][1] = *(const u32x4*)(a.cre + o + 8);
          cpi[u][0] = *(const u32x4*)(a.cim + o); cpi[u][1] = *(const u32x4*)(a.cim + o + 8);
        }
      }
    }
    if constexpr (s == 35 && ROLE == 0) {
      float vr = 0.f, vi = 0.f;
      clr = 0.f; cli = 0.f;
      const int sg = 2 * mq + h;
#pragma unroll
      for (int i = 0; i < 8; ++i) {
        f32x2 e = *(LDS3 f32x2*)(uintptr_t)(lb + L_SEG + (i * 32 + r) * 8);
        if (i == sg) { clr = vr; cli = vi; }
        float t = l16r * vr - l16i * vi + e[0];
        vi = l16r * vi + l16i * vr + e[1]; vr = t;
      }
      if (mq == 0) {
        u64 gv = (1ull << 32) | (u64)__float_as_uint(h ? vi : vr);
        __hip_atomic_store(aggp + (c * 14 + j) * 64 + lane, gv, __ATOMIC_RELAXED, __HIP_MEMORY_SCOPE_AGENT);
      }
    }
    if constexpr (s == 47 && ROLE == 0) {
#pragma unroll
      for (int i = 0; i < 15; ++i)
        gr[i] = __hip_atomic_load(aggp + (i * 14 + j) * 64 + lane, __ATOMIC_RELAXED, __HIP_MEMORY_SCOPE_AGENT);
    }
    if constexpr (s == 51) {
#pragma unroll
      for (int t = 0; t < (NH ? 3 : 2); ++t) cs_write(4 + (NH ? 2 : 0) + t, cs2[t]);
    }
    __builtin_amdgcn_sched_barrier(0);
  });

#pragma unroll
  for (int t = 0; t < (NH ? 3 : 2); ++t) {
    const int T = 2 + (NH ? 2 : 0) + t;
    acc2[t] = mfma(corr_w(cs_read(2 + T) + cs3[t]), cx, acc2[t]);
  }
#pragma unroll
  for (int t = 0; t < (NH ? 3 : 2); ++t) ffn_epi(acc2[t], 2 + (NH ? 2 : 0) + t);

  if constexpr (ROLE == 2) {
#pragma unroll
    for (int u = 0; u < 2; ++u) {
      int un = pq * 4 + mq + 128 * u;
      if (un < 196) {
        int cb = un / 14, jc = un - cb * 14;
#pragma unroll
        for (int q = 0; q < 4; ++q) {
          unsigned r0 = cpr[u][q >> 1][2 * (q & 1)], r1 = cpr[u][q >> 1][2 * (q & 1) + 1];
          unsigned i0 = cpi[u][q >> 1][2 * (q & 1)] ^ 0x80008000u, i1 = cpi[u][q >> 1][2 * (q & 1) + 1] ^ 0x80008000u;
          u32x4 o = {(r0 & 0xffffu) | (i0 << 16), (r0 >> 16) | (i0 & 0xffff0000u),
                     (r1 & 0xffffu) | (i1 << 16), (r1 >> 16) | (i1 & 0xffff0000u)};
          *(u32x4*)(wsB + (size_t)(cb * KS_ALL + 4 * jc + q) * 1024 + lane * 16) = o;
        }
      }
    }
  }
  if constexpr (ROLE == 0) {
    for (int spin = 0; spin < (1 << 18); ++spin) {
      bool ok = true;
#pragma unroll
      for (int i = 0; i < 15; ++i) ok &= (i >= c) || ((unsigned)(gr[i] >> 32) == 1u);
      if (__all(ok)) break;
      __builtin_amdgcn_s_sleep(2);
#pragma unroll
      for (int i = 0; i < 15; ++i)
        gr[i] = __hip_atomic_load(aggp + (i * 14 + j) * 64 + lane, __ATOMIC_RELAXED, __HIP_MEMORY_SCOPE_AGENT);
    }
    float l32r = l16r * l16r - l16i * l16i, l32i = 2.f * l16r * l16i;
    float l64r = l32r * l32r - l32i * l32i, l64i = 2.f * l32r * l32i;
    float lcr = l64r * l64r - l64i * l64i, lci = 2.f * l64r * l64i;
    float yr = 0.f, yi = 0.f;
#pragma unroll
    for (int i = 0; i < 15; ++i) {
      auto sw = __builtin_amdgcn_permlane32_swap((unsigned)gr[i], (unsigned)gr[i], false, false);
      float er = __uint_as_float(sw[0]), ei = __uint_as_float(sw[1]);
      float t = lcr * yr - lci * yi + er, t2 = lcr * yi + lci * yr + ei;
      if (i < c) { yr = t; yi = t2; }
    }
    if (h) { float t = l16r * yr - l16i * yi; yi = l16r * yi + l16i * yr; yr = t; }
    if (mq & 1) { float t = l32r * yr - l32i * yi; yi = l32r * yi + l32i * yr; yr = t; }
    if (mq & 2) { float t = l64r * yr - l64i * yi; yi = l64r * yi + l64i * yr; yr = t; }
    float hr = clr + yr, hi = cli + yi;
    const unsigned hb = lb + L_HT + mq * 4608;
#pragma unroll
    for (int i = 0; i < 16; ++i) {
      float t = lr * hr - li * hi + acc1[0][i];
      hi = lr * hi + li * hr + acc1[1][i]; hr = t;
      *(LDS3 unsigned*)(uintptr_t)(hb + ((i & 3) + 8 * (i >> 2) + 4 * h) * 144 + r * 4) = pack2(hr, hi);
    }
    asm volatile("" ::: "memory");
#pragma unroll
    for (int q = 0; q < 4; ++q) {
      u32x4 o = *(LDS3 u32x4*)(uintptr_t)(hb + r * 144 + (4 * h + q) * 16);
      *(u32x4*)(wsA + (size_t)(rb * KS_ALL + 4 * j + q) * 1024 + lane * 16) = o;
    }
  }
}

#define MAP1 0
__global__ __launch_bounds__(512, 2) void k1_kernel(K1Args a) {
  __shared__ __attribute__((aligned(16))) char smem[L_TOT];
  const unsigned lb = (unsigned)(uintptr_t)(LDS3 char*)smem;
  const int bid = blockIdx.x, xcd = bid & 7, idx = bid >> 3;
  int c, j;
  if (MAP1 == 0) { c = 2 * xcd + (idx >> 4); j = idx & 15; }
  else if (MAP1 == 1) { c = 4 * (xcd >> 1) + (idx >> 3); j = 8 * (xcd & 1) + (idx & 7); }
  else { c = 8 * (xcd >> 2) + (idx >> 2); j = 4 * (xcd & 3) + (idx & 3); }
  const int w = __builtin_amdgcn_readfirstlane(threadIdx.x >> 6), lane = threadIdx.x & 63;
  if (w >= 4) k1_body<1>(a, lb, c, j, w, lane);
  else if (j < 14) k1_body<0>(a, lb, c, j, w, lane);
  else k1_body<2>(a, lb, c, j, w, lane);
}

constexpr int PD = 10;
__global__ __launch_bounds__(512, 2) void k2_kernel(const char* __restrict__ ws, const unsigned short* __restrict__ x,
                                                    const unsigned short* __restrict__ dsk, float* __restrict__ out) {
  __shared__ __attribute__((aligned(16))) char smem[6 * 17408];
  const int bid = blockIdx.x, xcd = bid & 7, idx = bid >> 3;
  const int mt = 4 * xcd + idx / 7, nt = idx % 7;
  const int tid = threadIdx.x, w = __builtin_amdgcn_readfirstlane(tid >> 6), lane = tid & 63;
  if (w < 6) {
    const char* pa = ws + OFF_A + (size_t)(2 * mt * KS_ALL + 28 * w) * 1024 + lane * 16;
    const char* pb = ws + OFF_B + (size_t)(2 * nt * KS_ALL + 28 * w) * 1024 + lane * 16;
    bf16x8 fa[PD][2], fb[PD][2];
    f32x16 acc[2][2] = {};
    auto ld = [&](int s) {
      fa[s % PD][0] = *(const bf16x8*)(pa + s * 1024);
      fb[s % PD][0] = *(const bf16x8*)(pb + s * 1024);
      fa[s % PD][1] = *(const bf16x8*)(pa + (KS_ALL + s) * 1024);
      fb[s % PD][1] = *(const bf16x8*)(pb + (KS_ALL + s) * 1024);
    };
#pragma unroll
    for (int s = 0; s < PD; ++s) ld(s);
    __builtin_amdgcn_sched_barrier(0);
#pragma unroll
    for (int s = 0; s < 28; ++s) {
#pragma unroll
      for (int m = 0; m < 2; ++m)
#pragma unroll
        for (int n = 0; n < 2; ++n) acc[m][n] = mfma(fb[s % PD][n], fa[s % PD][m], acc[m][n]);
      if (s + PD < 28) ld(s + PD);
      __builtin_amdgcn_sched_barrier(0);
    }
    const int r = lane & 31, h = lane >> 5;
    char* pw = smem + w * 17408 + tau(r) * 272 + h * 16;
#pragma unroll
    for (int m = 0; m < 2; ++m)
#pragma unroll
      for (int n = 0; n < 2; ++n)
#pragma unroll
        for (int g = 0; g < 4; ++g) {
          f32x4 v = {acc[m][n][4 * g], acc[m][n][4 * g + 1], acc[m][n][4 * g + 2], acc[m][n][4 * g + 3]};
          *(f32x4*)(pw + m * 32 * 272 + n * 128 + g * 32) = v;
        }
  }
  const int c4 = tid & 15;
  u32x2 xv[2]; f32x2 st[2];
  const u32x2 dk = *(const u32x2*)(dsk + 64 * nt + 4 * c4);
#pragma unroll
  for (int e = 0; e < 2; ++e) {
    const int t = 64 * mt + (tid >> 4) + 32 * e;
    xv[e] = *(const u32x2*)(x + (size_t)t * DM + 64 * nt + 4 * c4);
    st[e] = *(const f32x2*)(ws + OFF_ST + (size_t)t * 8);
  }
  __syncthreads();
#pragma unroll
  for (int e = 0; e < 2; ++e) {
    const int row = (tid >> 4) + 32 * e;
    const char* pr = smem + row * 272 + c4 * 16;
    f32x4 sv = *(const f32x4*)(pr) + *(const f32x4*)(pr + 17408);
    f32x4 dv = (*(const f32x4*)(pr + 2 * 17408) + *(const f32x4*)(pr + 3 * 17408)) +
               (*(const f32x4*)(pr + 4 * 17408) + *(const f32x4*)(pr + 5 * 17408));
    f32x4 o;
#pragma unroll
    for (int i = 0; i < 4; ++i) {
      unsigned xw = xv[e][i >> 1], dw = dk[i >> 1];
      float xf = __uint_as_float((i & 1) ? (xw & 0xffff0000u) : (xw << 16));
      float df = __uint_as_float((i & 1) ? (dw & 0xffff0000u) : (dw << 16));
      float xn = (xf - st[e][0]) * st[e][1];
      o[i] = xf + (2.0f * sv[i] + xn * df) * silu(dv[i]);
    }
    *(f32x4*)(out + (size_t)(64 * mt + row) * DM + 64 * nt + 4 * c4) = o;
  }
}

extern "C" void kernel_launch(void* const* d_in, const int* in_sizes, int n_in, void* d_out, int out_size,
                              void* d_ws, size_t ws_size, hipStream_t stream) {
  K1Args a;
  a.x = (const unsigned short*)d_in[0];
  a.lre = (const unsigned short*)d_in[1];
  a.lim = (const unsigned short*)d_in[2];
  a.bre = (const unsigned short*)d_in[3];
  a.bim = (const unsigned short*)d_in[4];
  a.cre = (const unsigned short*)d_in[5];
  a.cim = (const unsigned short*)d_in[6];
  a.ldt = (const unsigned short*)d_in[7];
  a.wg = (const unsigned short*)d_in[10];
  a.wu = (const unsigned short*)d_in[11];
  a.wd = (const unsigned short*)d_in[12];
  a.ws = (char*)d_ws;
  (void)hipMemsetAsync((char*)d_ws + OFF_AGG, 0, AGG_BYTES, stream);
  k1_kernel<<<256, 512, 0, stream>>>(a);
  k2_kernel<<<224, 512, 0, stream>>>((const char*)d_ws, a.x, (const unsigned short*)d_in[8], (float*)d_out);
}
```

```cpp
#include <hip/hip_runtime.h>
#include <hip/hip_bf16.h>
#include <stdint.h>
#include <utility>

typedef __attribute__((ext_vector_type(8))) __bf16 bf16x8;
typedef __attribute__((ext_vector_type(4))) __bf16 bf16x4;
typedef __attribute__((ext_vector_type(2))) __bf16 bf16x2;
typedef __attribute__((ext_vector_type(16))) float f32x16;
typedef __attribute__((ext_vector_type(4))) float f32x4;
typedef __attribute__((ext_vector_type(2))) float f32x2;
typedef __attribute__((ext_vector_type(4))) unsigned u32x4;
typedef __attribute__((ext_vector_type(2))) unsigned u32x2;
typedef unsigned long long u64;
#define LDS3 __attribute__((address_space(3)))
#define GLB1 __attribute__((address_space(1)))
#define DEVI __device__ __forceinline__

constexpr int DM = 448, FF = 1792;
constexpr int KS_ALL = 168;
constexpr size_t OFF_AGG = 0;
constexpr size_t OFF_FLG = 224 * 8192;
constexpr size_t OFF_A = 2097152;

template <int... Is, class Fn>
DEVI void static_for_impl(std::integer_sequence<int, Is...>, Fn&& f) {
  (f(std::integral_constant<int, Is>{}), ...);
}
template <int N, class Fn>
DEVI void static_for(Fn&& f) { static_for_impl(std::make_integer_sequence<int, N>{}, f); }

DEVI void glds2(unsigned v0, unsigned v1, const void* sbase, unsigned ldsdst) {
  unsigned keep;
  asm volatile("s_mov_b32 %0, m0\n\ts_mov_b32 m0, %4\n\ts_nop 0\n\tglobal_load_lds_dwordx4 %1, %3\n\t"
               "global_load_lds_dwordx4 %2, %3 offset:1024\n\ts_mov_b32 m0, %0"
               : "=&s"(keep) : "v"(v0), "v"(v1), "s"(sbase), "s"(ldsdst) : "memory");
}
DEVI void glds1(unsigned v0, const void* sbase, unsigned ldsdst) {
  unsigned keep;
  asm volatile("s_mov_b32 %0, m0\n\ts_mov_b32 m0, %3\n\ts_nop 0\n\tglobal_load_lds_dwordx4 %1, %2\n\ts_mov_b32 m0, %0"
               : "=&s"(keep) : "v"(v0), "s"(sbase), "s"(ldsdst) : "memory");
}
DEVI void st16(unsigned vo, const void* sb, u32x4 v) {
  asm volatile("global_store_dwordx4 %0, %1, %2 sc1\n\ts_nop 1" ::"v"(vo), "v"(v), "s"(sb) : "memory");
}
template <int N> DEVI void wait_vm_lgkm0() { asm volatile("s_waitcnt vmcnt(%0) lgkmcnt(0)" ::"n"(N) : "memory"); }
DEVI float bf2f(unsigned short v) { return __uint_as_float((unsigned)v << 16); }
DEVI unsigned pack2(float a, float b) {
  f32x2 p = {a, b};
  return __builtin_bit_cast(unsigned, __builtin_convertvector(p, bf16x2));
}
DEVI float dot2(unsigned a, unsigned b, float c) {
  return __builtin_amdgcn_fdot2_f32_bf16(__builtin_bit_cast(bf16x2, a), __builtin_bit_cast(bf16x2, b), c, false);
}
DEVI bf16x8 lds_b128(unsigned addr) { return *(LDS3 bf16x8*)(uintptr_t)addr; }
DEVI bf16x8 lds_tr(unsigned a0, unsigned a1) {
  bf16x4 a = __builtin_amdgcn_ds_read_tr16_b64_v4bf16((LDS3 bf16x4*)(uintptr_t)a0);
  bf16x4 b = __builtin_amdgcn_ds_read_tr16_b64_v4bf16((LDS3 bf16x4*)(uintptr_t)a1);
  return __builtin_shufflevector(a, b, 0, 1, 2, 3, 4, 5, 6, 7);
}
DEVI f32x16 mfma(bf16x8 a, bf16x8 b, f32x16 c) { return __builtin_amdgcn_mfma_f32_32x32x16_bf16(a, b, c, 0, 0, 0); }
DEVI float silu(float g) { return g * __builtin_amdgcn_rcpf(1.0f + __expf(-g)); }

template <int OFF> DEVI void gld16(u32x4& d, unsigned vo, const void* sb) {
  asm volatile("global_load_dwordx4 %0, %1, %2 offset:%3" : "=v"(d) : "v"(vo), "s"(sb), "n"(OFF) : "memory");
}
#define ACQ 0
#define WARM2 1
DEVI void gld16_sc1(u32x4& d, unsigned vo, const void* sb) {
  asm volatile("global_load_dwordx4 %0, %1, %2 sc1" : "=v"(d) : "v"(vo), "s"(sb) : "memory");
}
template <int N> DEVI void wait4(u32x4 (&g)[4]) {
  asm volatile("s_waitcnt vmcnt(%4)" : "+v"(g[0]), "+v"(g[1]), "+v"(g[2]), "+v"(g[3]) : "n"(N) : "memory");
}
template <int N> DEVI void wait2(u32x4& x, u32x4& y) {
  asm volatile("s_waitcnt vmcnt(%2)" : "+v"(x), "+v"(y) : "n"(N) : "memory");
}
template <int N> DEVI void wait7(u32x4 (&g)[7]) {
  asm volatile("s_waitcnt vmcnt(%7)" : "+v"(g[0]), "+v"(g[1]), "+v"(g[2]), "+v"(g[3]), "+v"(g[4]), "+v"(g[5]), "+v"(g[6]) : "n"(N) : "memory");
}
DEVI void keepv(float& v) { asm volatile("" : "+v"(v)); }
DEVI void keepi(int& v) { asm volatile("" : "+v"(v)); }
DEVI void useu(unsigned v) { asm volatile("" ::"v"(v)); }
DEVI void gld4(unsigned& d, unsigned vo, const void* sb) { asm volatile("global_load_dword %0, %1, %2" : "=v"(d) : "v"(vo), "s"(sb) : "memory"); }
DEVI void cbar() { asm volatile("" ::: "memory"); }
template <int N> DEVI void wait_vm() { asm volatile("s_waitcnt vmcnt(%0)" ::"n"(N) : "memory"); }
DEVI void bar_lgkm() { asm volatile("s_waitcnt lgkmcnt(0)\n\ts_barrier" ::: "memory"); }
template <int N> DEVI void bar_vm() { asm volatile("s_waitcnt vmcnt(%0) lgkmcnt(0)\n\ts_barrier" ::"n"(N) : "memory"); }
template <class T> DEVI T ldsr(unsigned a) { return *(LDS3 T*)(uintptr_t)a; }
template <class T> DEVI void ldsw(unsigned a, T v) { *(LDS3 T*)(uintptr_t)a = v; }
DEVI int tau(int r) { return 16 * ((r >> 2) & 1) + (r & 3) + 4 * (r >> 3); }
template <int C> DEVI float dpp_add(float v) {
  return v + __uint_as_float(__builtin_amdgcn_update_dpp(0u, __float_as_uint(v), C, 0xf, 0xf, true));
}
DEVI void cmul(float& ar, float& ai, float br, float bi) { float t = ar * br - ai * bi; ai = ar * bi + ai * br; ar = t; }
DEVI void csq(float ar, float ai, float& br, float& bi) { br = ar * ar - ai * ai; bi = 2.f * ar * ai; }
DEVI void swap16(float v, float& even, float& odd) {
  auto s = __builtin_amdgcn_permlane16_swap(__float_as_uint(v), __float_as_uint(v), false, false);
  even = __uint_as_float(s[0]); odd = __uint_as_float(s[1]);
}
DEVI void swap32(float v, float& lo, float& hi) {
  auto s = __builtin_amdgcn_permlane32_swap(__float_as_uint(v), __float_as_uint(v), false, false);
  lo = __uint_as_float(s[0]); hi = __uint_as_float(s[1]);
}

#define TS(i)

constexpr int SLOT = 16384;
constexpr int L_D3 = 9 * SLOT;
constexpr int L_SEG = L_D3 + 8192;
constexpr int L_PAR = L_SEG + 1024;
constexpr int L_RS = L_PAR + 512;
constexpr int L_TOT = L_RS + 1024;
constexpr int SPIN = 1 << 18;

struct Args {
  const unsigned short *x, *lre, *lim, *bre, *bim, *cre, *cim, *ldt, *dsk, *wg, *wu, *wd;
  char* ws;
  float* out;
};

constexpr int kstep_of(int s) { return s < 16 ? s : (s < 20 ? s + 8 : (s < 28 ? s - 4 : (s - 28) % 28)); }
constexpr int wslot(int n) { return (n + 1) % 9; }
constexpr int PA = 6;
constexpr int young2(int s) {
  int n = 2 * ((s + PA - 1 < 27 ? s + PA - 1 : 27) - s);
  if (s >= 5 && s <= 4 + PA) n += 8;
  return n;
}

__global__ __launch_bounds__(512, 2) void fused_kernel(Args a) {
  __shared__ __attribute__((aligned(16))) char smem[L_TOT];
  const unsigned lb = (unsigned)(uintptr_t)(LDS3 char*)smem;
  const int xcd = blockIdx.x & 7, idx = blockIdx.x >> 3;
  const int w = __builtin_amdgcn_readfirstlane(threadIdx.x >> 6), lane = threadIdx.x & 63;
  if (idx >= 28) {
    unsigned acc = 0;
    const int p = (idx - 28) * 8 + w;
#pragma unroll 2
    for (int it = 0; it < 10; ++it) {
      int ln = min((it * 32 + p) * 64 + lane, 18815), qd = ln / 2688, q2 = ln - qd * 2688, kr = q2 / 448, q3 = q2 - kr * 448;
      const unsigned short* src = kr >= 2 ? a.wd + (size_t)(64 * (7 * (kr - 2) + qd)) * DM + q3 * 64
                                          : (kr ? a.cim : a.cre) + (size_t)q3 * DM + qd * 64;
      acc ^= *(const unsigned*)src;
    }
    useu(acc);
    return;
  }
  const int nt = idx % 7, jg = xcd & 1, c = 4 * (xcd >> 1) + idx / 7, j = 7 * jg + nt;
  const int mq = w & 3, nh = w >> 2;
  const int r = lane & 31, h = lane >> 5, o16 = (lane >> 4) & 1, c16 = lane & 15, dist = lane >> 4;
  const char* const wsA = a.ws + OFF_A + (size_t)((4 * c + mq) * KS_ALL) * 1024;
  TS(0);
  unsigned p_re = 0, p_im = 0, p_dt = 0;
  u32x4 raw[2][7];
  if (w == 0) {
    asm volatile("global_load_ushort %0, %1, %2" : "=v"(p_re) : "v"((32 * j + r) * 2), "s"(a.lre) : "memory");
    asm volatile("global_load_ushort %0, %1, %2" : "=v"(p_im) : "v"((32 * j + r) * 2), "s"(a.lim) : "memory");
    asm volatile("global_load_ushort %0, %1, %2" : "=v"(p_dt) : "v"((32 * j + r) * 2), "s"(a.ldt) : "memory");
  }
  int rhog[2];
  const char* const xb = (const char*)a.x + (size_t)(128 * c) * 896;
#pragma unroll
  for (int g = 0; g < 2; ++g) {
    const int rho = 8 * ((8 * nh + 4 * g + mq + 2 * nt + 1) & 15) + (lane >> 3);
    rhog[g] = rho;
    const unsigned vo = ((rho & 96) + tau(rho & 31)) * 896 + (lane & 7) * 16;
    static_for<7>([&](auto I) { gld16<I * 128>(raw[g][I], vo, xb); });
  }
  const unsigned vq = (lane >> 3) * 896 + (((lane & 7) ^ ((4 * w + (lane >> 4)) & 7)) << 4);
  const char* const bb = (const char*)((w & 2) ? a.bim : a.bre) + (32 * j + 8 * (w & 1)) * 896;
  const char* const wb = (const char*)(nh ? a.wu : a.wg) + 256 * j;
  const char* const inbox = a.ws + OFF_AGG + (size_t)(c * 14 + j) * 8192;
  unsigned warm = 0, warm2 = 0;
  if (nh == 1) {
#pragma unroll
    for (int i = 0; i < 5; ++i) {
      const int slab = i < 4 ? i : 6;
      glds1(vq, bb + slab * 128, lb + 7 * SLOT + i * 8192 + mq * 1024);
      glds1(vq, bb + slab * 128 + 16 * 896, lb + 7 * SLOT + i * 8192 + mq * 1024 + 4096);
    }
  }
  TS(12);
#pragma unroll
  for (int g = 0; g < 2; ++g) {
    if (nh == 0) { if (g == 0) wait_vm<7>(); else wait_vm<0>(); }
    if (g == 0) wait7<17>(raw[0]); else wait7<10>(raw[1]);
    if (g == 0) { TS(1); } else { TS(14); }
    float s1a = 0.f, s2a = 0.f, s1b = 0.f, s2b = 0.f;
#pragma unroll
    for (int i = 0; i < 7; ++i) {
      s1a = dot2(raw[g][i][0], 0x3f803f80u, s1a); s2a = dot2(raw[g][i][0], raw[g][i][0], s2a);
      s1b = dot2(raw[g][i][1], 0x3f803f80u, s1b); s2b = dot2(raw[g][i][1], raw[g][i][1], s2b);
      s1a = dot2(raw[g][i][2], 0x3f803f80u, s1a); s2a = dot2(raw[g][i][2], raw[g][i][2], s2a);
      s1b = dot2(raw[g][i][3], 0x3f803f80u, s1b); s2b = dot2(raw[g][i][3], raw[g][i][3], s2b);
    }
    float s1 = s1a + s1b, s2 = s2a + s2b;
    s1 = dpp_add<0xB1>(s1); s2 = dpp_add<0xB1>(s2);
    s1 = dpp_add<0x4E>(s1); s2 = dpp_add<0x4E>(s2);
    s1 = dpp_add<0x141>(s1); s2 = dpp_add<0x141>(s2);
    const float mean = s1 * (1.0f / DM);
    const float rstd = __builtin_amdgcn_rsqf(fmaxf(s2 * (1.0f / DM) - mean * mean, 0.f) + 1e-5f);
    const float nb = -mean * rstd;
    const int rho = rhog[g];
    const unsigned wa = lb + rho * 128 + (((lane & 7) ^ ((rho >> 1) & 7)) << 4);
#pragma unroll
    for (int i = 0; i < 7; ++i) {
      u32x4 ov;
#pragma unroll
      for (int e = 0; e < 4; ++e) {
        unsigned u = raw[g][i][e];
        ov[e] = pack2(fmaf(__uint_as_float(u << 16), rstd, nb), fmaf(__uint_as_float(u & 0xffff0000u), rstd, nb));
      }
      ldsw(wa + i * SLOT, ov);
    }
    ldsw(lb + L_RS + rho * 8, f32x2{rstd, nb});
  }
  TS(2);
  {
    int ln = min(mq * 64 + lane, 223) * 4 + (c & 3), un = ln >= 448, row = ln - 448 * un;
    asm volatile("global_load_dword %0, %1, %2" : "=v"(warm) : "v"(row * 3584 + un * 128), "s"(wb) : "memory");
  }
  if (w == 0) {
    asm volatile("" : "+v"(p_re), "+v"(p_im), "+v"(p_dt));
    float dt = expf(bf2f(p_dt)), are = bf2f(p_re), aim = bf2f(p_im);
    float e = expf(dt * are), sn, cn;
    sincosf(dt * aim, &sn, &cn);
    float lr = e * cn, li = e * sn;
    float inv = 1.0f / (are * are + aim * aim);
    float nr = lr - 1.0f, ni = li;
    f32x4 pv = {lr, li, (nr * are + ni * aim) * inv, (ni * are - nr * aim) * inv};
    ldsw(lb + L_PAR + r * 16, pv);
  }
  TS(13);
  auto stage_b = [&](int slab, unsigned dst) { glds1(vq, bb + slab * 128 + nh * (16 * 896), lb + dst + w * 1024); };
  const unsigned vw0 = (16 * mq + (lane >> 3)) * 3584 + (((lane & 7) ^ (2 * (2 * nh + ((lane >> 4) & 1)))) << 4);
  const unsigned vw1 = vw0 + 8 * 3584 - 1024;
  auto stage_w = [&](int n) {
    glds2(vw0, vw1, wb + (n / 7) * 128 + (n % 7) * (64 * 3584), lb + wslot(n) * SLOT + w * 2048);
  };
  auto quad_src = [&](int kr, int qd) {
    return kr >= 2 ? (const char*)a.wd + (size_t)(64 * (7 * (kr - 2) + qd)) * 896 + nt * 128
                   : (const char*)(kr ? a.cim : a.cre) + (size_t)(64 * nt) * 896 + qd * 128;
  };
  auto stage_p = [&](int sl) {
#pragma unroll
    for (int e = 0; e < 2; ++e) {
      const int u = 2 * sl + e;
      glds1(vq, quad_src(u / 3, u % 3) + w * (8 * 896), lb + u * 8192 + w * 1024);
    }
  };

  unsigned toff[4];
#pragma unroll
  for (int ks = 0; ks < 4; ++ks) toff[ks] = lb + r * 128 + (((2 * ks + h) ^ ((r >> 1) & 7)) << 4);
  const int q4 = (lane >> 2) & 3;
  unsigned wta[2][3];
#pragma unroll
  for (int t = 0; t < 2; ++t) {
    wta[t][0] = lb + o16 * 8192 + (8 * h + q4) * 128 + (((2 * nh + t) ^ (2 * o16 + (q4 >> 1))) << 5) + (lane & 3) * 8;
    wta[t][1] = wta[t][0] + 3 * SLOT; wta[t][2] = wta[t][0] + 6 * SLOT;
  }

  {
  bf16x8 xf[28];
  bf16x8 fs[4], fr[3][2];
  f32x16 accs = {}, accu[2][2] = {};
  float lr, li, cfr, cfi, l64r, l64i, lcr, lci, l2r, l2i, l4r, l4i, yr = 0.f, yi = 0.f, t0r, t0i, t1r, t1i;
  float l8r, l8i, l16r, l16i, l32r, l32i, clr = 0.f, cli = 0.f, er = 0.f, ei = 0.f;
  float ur[8], ui[8];
  u32x4 gr[4], ov[2];
  float pbr, pbi;
  f32x2 sg[4];
  const char* const pubb = a.ws + OFF_AGG + (4 * mq * 14 + j) * 8192 + c * 512 + nh * 256;
  auto publish = [&]() {
    int l2 = lane; keepi(l2);
    st16((l2 >> 4) * (14 * 8192) + (l2 & 15) * 16, pubb, u32x4{__float_as_uint(pbr), 1u, __float_as_uint(pbi), 1u});
  };

  auto load_x = [&](auto KI) {
    constexpr int k = KI;
    xf[k] = lds_b128(toff[k & 3] + mq * 4096 + (k >> 2) * SLOT);
  };
  auto load_s = [&](auto SI) {
    constexpr int s = SI, k = kstep_of(s), sl = k >> 2;
    constexpr int off = sl < 4 ? 7 * SLOT + sl * 8192 : (sl == 6 ? L_D3 : (sl - 4) * 8192);
    fs[s & 3] = lds_b128(toff[k & 3] + nh * 4096 + off);
  };
  auto load_w = [&](auto SI, int t) {
    constexpr int s = SI;
    if constexpr (s < 84) {
      constexpr int n = (s - 28) >> 2, ks = s & 3, sl = wslot(n);
      unsigned ad = wta[t][sl / 3] + (sl % 3) * SLOT + ks * 2048;
      fr[s % 3][t] = lds_tr(ad, ad + 512);
    }
  };
  auto poll = [&]() {
    int l2 = lane; keepi(l2);
#pragma unroll
    for (int n = 0; n < 4; ++n) gld16_sc1(gr[n], ((c - 1 - 4 * n - (l2 >> 4)) & 15) * 512 + (l2 & 15) * 16, inbox + nh * 256);
  };
  auto act1 = [&](float g, float u) { return (g * u) * __builtin_amdgcn_rcpf(1.0f + __builtin_amdgcn_exp2f(g * -1.44269504f)); };
  auto epi2 = [&](const f32x16& ac, int i) { return pack2(act1(ac[2 * i], ac[8 + 2 * i]), act1(ac[2 * i + 1], ac[9 + 2 * i])); };

  auto hook = [&](auto QI) {
    constexpr int q = QI;
    if constexpr (q >= 2 && q < 18) {
      constexpr int i = (q - 2) >> 1;
      if constexpr ((q & 1) == 0) {
        auto sw = __builtin_amdgcn_permlane16_swap(__float_as_uint(accs[i]), __float_as_uint(accs[i + 8]), false, false);
        float vr = __uint_as_float(sw[0]), vi = __uint_as_float(sw[1]);
        ur[i] = cfr * vr - cfi * vi; ui[i] = cfr * vi + cfi * vr;
      } else {
        float t = lr * er - li * ei + ur[i];
        ei = lr * ei + li * er + ui[i]; er = t;
      }
    }
    if constexpr (q == 13) stage_w(9);
    if constexpr (q == 18) { swap16(er, t0r, yr); swap16(ei, t0i, yi); }
    if constexpr (q == 19) { er = l8r * t0r - l8i * t0i + yr; ei = l8r * t0i + l8i * t0r + yi; }
    if constexpr (q == 20) { swap32(er, t1r, yr); swap32(ei, t1i, yi); }
    if constexpr (q == 21) {
      ldsw(lb + L_SEG + ((nh * 4 + mq) * 16 + c16) * 8, f32x2{l16r * t1r - l16i * t1i + yr, l16r * t1i + l16i * t1r + yi});
    }
    if constexpr (q == 22) stage_w(10);
    if constexpr (q == 23) { t1r = h ? t1r : 0.f; t1i = h ? t1i : 0.f; yr = l8r * t1r - l8i * t1i + t0r; yi = l8r * t1i + l8i * t1r + t0i; }
    if constexpr (q == 24) { clr = o16 ? yr : t1r; cli = o16 ? yi : t1i; csq(l32r, l32i, l64r, l64i); }
    if constexpr (q == 25) { yr = h ? l16r : 1.f; yi = h ? l16i : 0.f; t1r = o16 ? l8r : 1.f; t1i = o16 ? l8i : 0.f; }
    if constexpr (q == 26) cmul(yr, yi, t1r, t1i);
    if constexpr (q == 28) {
#pragma unroll
      for (int i = 0; i < 4; ++i) sg[i] = ldsr<f32x2>(lb + L_SEG + ((nh * 4 + i) * 16 + c16) * 8);
    }
    if constexpr (q == 29) stage_w(11);
    if constexpr (q == 30) { t0r = l32r * sg[0][0] - l32i * sg[0][1] + sg[1][0]; t0i = l32r * sg[0][1] + l32i * sg[0][0] + sg[1][1]; }
    if constexpr (q == 31) { t1r = l32r * t0r - l32i * t0i + sg[2][0]; t1i = l32r * t0i + l32i * t0r + sg[2][1]; }
    if constexpr (q == 32) { pbr = l32r * t1r - l32i * t1i + sg[3][0]; pbi = l32r * t1i + l32i * t1r + sg[3][1]; }
    if constexpr (q == 33) publish();
    if constexpr (q == 34) {
      er = mq == 0 ? 0.f : (mq == 1 ? sg[0][0] : (mq == 2 ? t0r : t1r));
      ei = mq == 0 ? 0.f : (mq == 1 ? sg[0][1] : (mq == 2 ? t0i : t1i));
    }
    if constexpr (q == 35) { clr += yr * er - yi * ei; cli += yr * ei + yi * er; }
    if constexpr (q == 36) { t1r = (mq & 1) ? l32r : 1.f; t1i = (mq & 1) ? l32i : 0.f; cmul(yr, yi, t1r, t1i); }
    if constexpr (q == 37) { t0r = (mq & 2) ? l64r : 1.f; t0i = (mq & 2) ? l64i : 0.f; cmul(t0r, t0i, yr, yi); }
    if constexpr (q == 38) stage_w(12);
    if constexpr (q == 45) stage_w(13);
    if constexpr (q >= 58 && q < 78 && q != 61 && q != 66 && q != 70 && q != 74) {
      constexpr int e = q - 58 - (q > 61) - (q > 66) - (q > 70) - (q > 74), t = e >> 3, i = e & 7;
      if constexpr ((i & 1) == 0) t1r = act1(accu[0][t][i], accu[0][t][8 + i]);
      else ov[t][i >> 1] = pack2(t1r, act1(accu[0][t][i], accu[0][t][8 + i]));
      if constexpr (i == 7) st16(lane * 16, wsA + (56 + 8 * j + 2 * nh + t) * 1024, ov[t]);
    }
    if constexpr (q == 61) stage_p(7);
    if constexpr (q == 66) stage_p(8);
    if constexpr (q == 70) poll();
    if constexpr (q == 74) { csq(l64r, l64i, lcr, lci); csq(lcr, lci, l2r, l2i); }
    if constexpr (q == 78) stage_p(0);
    if constexpr (q == 79) csq(l2r, l2i, l4r, l4i);
    if constexpr (q == 80) {
      wait4<3>(gr);
      for (int spin = 0; spin < SPIN; ++spin) {
        unsigned ok = (gr[0][1] & gr[0][3] & gr[1][1]) & (gr[1][3] & gr[2][1] & gr[2][3]) & (gr[3][1] & gr[3][3]);
        if (__all(ok != 0u)) break;
        __builtin_amdgcn_s_sleep(2);
        poll();
        wait4<0>(gr);
      }
      TS(29);
    }
    if constexpr (q == 81) { keepv(pbr); publish(); }
    if constexpr (q >= 82 && q < 86) {
      constexpr int n = 85 - q;
      bool v = 4 * n + dist < c;
      float xr = v ? __uint_as_float(gr[n][0]) : 0.f, xi = v ? __uint_as_float(gr[n][2]) : 0.f;
      if constexpr (n == 3) { yr = xr; yi = xi; }
      else { float t = l4r * yr - l4i * yi + xr; yi = l4r * yi + l4i * yr + xi; yr = t; }
    }
    if constexpr (q == 86) stage_p(1);
    if constexpr (q == 87) { swap16(yr, er, t1r); swap16(yi, ei, t1i); }
    if constexpr (q == 88) { yr = er + lcr * t1r - lci * t1i; yi = ei + lcr * t1i + lci * t1r; }
    if constexpr (q == 89) { swap32(yr, er, t1r); swap32(yi, ei, t1i); }
    if constexpr (q == 90) { yr = er + l2r * t1r - l2i * t1i; yi = ei + l2r * t1i + l2i * t1r; }
    if constexpr (q == 91) { er = clr + t0r * yr - t0i * yi; ei = cli + t0r * yi + t0i * yr; }
    if constexpr (q == 92) stage_p(2);
    if constexpr (q >= 93 && q < 101) {
      constexpr int i = q - 93;
      float t = lr * er - li * ei + ur[i];
      ei = lr * ei + li * er + ui[i]; er = t;
      ldsw(lb + 6 * SLOT + w * 2048 + ((i & 3) + 8 * (i >> 2) + 16 * o16 + 4 * h) * 64 + ((c16 * 4) ^ (((2 * (i >> 2) + h) & 3) << 4)), pack2(er, -ei));
    }
    if constexpr (q == 101) {
      cbar();
#pragma unroll
      for (int e = 0; e < 2; ++e) ov[e] = ldsr<u32x4>(lb + 6 * SLOT + w * 2048 + r * 64 + (((2 * h + e) ^ ((r >> 2) & 3)) << 4));
    }
    if constexpr (q == 102) stage_p(3);
    if constexpr (q == 104 || q == 105) {
      u32x4 o;
#pragma unroll
      for (int i = 0; i < 4; ++i)
        o[i] = __builtin_amdgcn_perm(ov[i >> 1][2 * (i & 1) + 1], ov[i >> 1][2 * (i & 1)], q == 104 ? 0x05040100u : 0x07060302u);
      st16(lane * 16, wsA + ((q - 104) * 28 + 2 * j + nh) * 1024, o);
    }
    if constexpr (q >= 108 && q < 111) stage_p(q - 104);
  };

  bar_vm<1>();
  TS(3);
  static_for<4>(load_x);
  static_for<3>(load_s);

  static_for<84>([&](auto SI) {
    constexpr int s = SI;
    if constexpr (s == 3 || s == 6) { bar_lgkm(); TS(s == 3 ? 4 : 5); }
    if constexpr (s == 17) { bar_vm<10>(); useu(warm); TS(6); }
    if constexpr (s == 26) { bar_vm<14>(); TS(7); }
    if constexpr (s == 34) { bar_vm<10>(); TS(8); }
    if constexpr (s == 42) { bar_vm<10>(); TS(9); }
    if constexpr (s == 50) { bar_vm<11>(); TS(10); }
    if constexpr (s == 58) { bar_vm<0>(); TS(11); }
    if constexpr (s == 66 || s == 74 || s == 82) { bar_lgkm(); TS(26 + (s - 66) / 8); }
    if constexpr (s < 28) {
      if constexpr (s < 6) static_for<4>([&](auto I) { load_x(std::integral_constant<int, 4 * (s + 1) + I>{}); });
      if constexpr (s + 3 < 28) load_s(std::integral_constant<int, s + 3>{});
      if constexpr (s >= 26) { load_w(std::integral_constant<int, s + 2>{}, 0); load_w(std::integral_constant<int, s + 2>{}, 1); }
      __builtin_amdgcn_sched_barrier(0);
      accs = mfma(xf[kstep_of(s)], fs[s & 3], accs);
      __builtin_amdgcn_sched_barrier(0);
      if constexpr (s == 3) { stage_b(4, 0); stage_b(5, 8192); }
      if constexpr (s == 5) stage_w(0);
      if constexpr (s == 8) stage_w(1);
      if constexpr (s == 11) stage_w(2);
      if constexpr (s == 13) {
        u32x4 z = {0u, 0u, 0u, 0u};
        st16(w == 6 && lane < 8 ? (unsigned)(OFF_FLG - OFF_AGG) + lane * 16 - (c * 14 + j) * 8064 : w * 1024 + lane * 16, inbox, z);
        st16(w * 1024 + lane * 16, inbox, z);
      }
      if constexpr (s == 15) stage_w(3);
      if constexpr (s == 18 || s == 20 || s == 22 || s == 24) stage_w(4 + (s - 18) / 2);
      if constexpr (s == 27) stage_w(8);
      if constexpr (s == 16) {
        f32x4 pv = ldsr<f32x4>(lb + L_PAR + (16 * nh + c16) * 16);
        lr = pv[0]; li = pv[1]; cfr = pv[2]; cfi = pv[3];
      }
      if constexpr (s == 19) { csq(lr, li, t0r, t0i); csq(t0r, t0i, t1r, t1i); }
      if constexpr (s == 21) { csq(t1r, t1i, l8r, l8i); csq(l8r, l8i, l16r, l16i); }
      if constexpr (s == 23) csq(l16r, l16i, l32r, l32i);
      __builtin_amdgcn_sched_barrier(0);
    } else {
      constexpr int u = (s - 28) / 28;
      accu[u][0] = mfma(fr[s % 3][0], xf[kstep_of(s)], accu[u][0]);
      __builtin_amdgcn_sched_barrier(0);
      load_w(std::integral_constant<int, s + 2>{}, 0);
      hook(std::integral_constant<int, 2 * (s - 28)>{});
      __builtin_amdgcn_sched_barrier(0);
      accu[u][1] = mfma(fr[s % 3][1], xf[kstep_of(s)], accu[u][1]);
      __builtin_amdgcn_sched_barrier(0);
      load_w(std::integral_constant<int, s + 2>{}, 1);
      hook(std::integral_constant<int, 2 * (s - 28) + 1>{});
      __builtin_amdgcn_sched_barrier(0);
    }
  });
  TS(15);
#pragma unroll
  for (int t = 0; t < 2; ++t) {
#pragma unroll
    for (int i = 0; i < 4; ++i) ov[t][i] = epi2(accu[1][t], i);
    st16(lane * 16, wsA + (56 + 8 * j + 4 + 2 * nh + t) * 1024, ov[t]);
  }
  }
  TS(16);
  bar_vm<0>();
  TS(17);

  int tid = threadIdx.x;
  asm volatile("" : "+v"(tid));
  const int c4 = tid & 15;
  u32x2 xv[2];
  const u32x2 dk = *(const u32x2*)(a.dsk + 64 * nt + 4 * c4);
  int trow[2];
#pragma unroll
  for (int e = 0; e < 2; ++e) {
    trow[e] = 128 * c + 64 * jg + 32 * e + tau(tid >> 4);
    xv[e] = *(const u32x2*)(a.x + (size_t)trow[e] * DM + 64 * nt + 4 * c4);
  }
  if (w < 6) {
    const int lane = tid & 63, r = lane & 31, h = lane >> 5, o16 = (lane >> 4) & 1, q4 = (lane >> 2) & 3;
    unsigned toff[4];
#pragma unroll
    for (int ks = 0; ks < 4; ++ks) toff[ks] = lb + r * 128 + (((2 * ks + h) ^ ((r >> 1) & 7)) << 4);
    bf16x8 bq[3][4][2];
    u32x4 af[PA][2];
    f32x16 acc[2][2] = {};
    const unsigned rg = w * 24576;
    unsigned tra[2][2];
#pragma unroll
    for (int n = 0; n < 2; ++n)
#pragma unroll
      for (int e = 0; e < 2; ++e)
        tra[n][e] = lb + rg + (4 * h + q4 + 8 * e) * 128 + (((4 * n + 2 * o16 + ((lane >> 1) & 1)) ^ ((2 * h + (q4 >> 1) + 4 * e) & 7)) << 4) + (lane & 1) * 8;
    auto read_quad = [&](int u) {
      if (w < 2) {
#pragma unroll
        for (int i = 0; i < 4; ++i)
#pragma unroll
          for (int n = 0; n < 2; ++n) bq[u][i][n] = lds_b128(toff[i] + rg + u * 8192 + n * 4096);
      } else {
#pragma unroll
        for (int i = 0; i < 4; ++i)
#pragma unroll
          for (int n = 0; n < 2; ++n) bq[u][i][n] = lds_tr(tra[n][0] + u * 8192 + i * 2048, tra[n][1] + u * 8192 + i * 2048);
      }
    };
    const unsigned vq0 = (lane >> 3) * 896 + (((lane & 7) ^ (lane >> 4)) << 4);
    const unsigned vq1 = ((lane >> 3) + 8) * 896 + (((lane & 7) ^ (4 + (lane >> 4))) << 4) - 1024;
    auto stage_quad = [&](int qd) {
      const char* src = quad_src(w, qd);
#pragma unroll
      for (int i = 0; i < 4; ++i) glds2(vq0, vq1, src + i * (16 * 896), lb + rg + (qd % 3) * 8192 + i * 2048);
    };
    read_quad(0); read_quad(1); read_quad(2);
    asm volatile("s_waitcnt lgkmcnt(0)" ::: "memory");
    __builtin_amdgcn_sched_barrier(0);
    stage_quad(3); stage_quad(4); stage_quad(5);
    TS(20);
    __builtin_amdgcn_s_barrier();
    TS(21);
    const char* const pa = a.ws + OFF_A + (size_t)((4 * c + 2 * jg) * KS_ALL + 28 * w) * 1024;
    const unsigned l0 = lane * 16, l1 = lane * 16 + KS_ALL * 1024;
    auto lda = [&](int s) {
      if (ACQ) { gld16<0>(af[s % PA][0], l0, pa + s * 1024); gld16<0>(af[s % PA][1], l1, pa + s * 1024); }
      else { gld16_sc1(af[s % PA][0], l0, pa + s * 1024); gld16_sc1(af[s % PA][1], l1, pa + s * 1024); }
    };
#pragma unroll
    for (int s = 0; s < PA; ++s) lda(s);
    static_for<28>([&](auto SI) {
      constexpr int s = SI;
      wait2<young2(s)>(af[s % PA][0], af[s % PA][1]);
#pragma unroll
      for (int m = 0; m < 2; ++m)
#pragma unroll
        for (int n = 0; n < 2; ++n)
          acc[m][n] = mfma(bq[(s >> 2) % 3][s & 3][n], __builtin_bit_cast(bf16x8, af[s % PA][m]), acc[m][n]);
      __builtin_amdgcn_sched_barrier(0);
      if constexpr (s + PA < 28) lda(s + PA);
      if constexpr ((s & 3) == 3 && s < 16) read_quad((s >> 2) % 3);
      if constexpr (s == 4) { asm volatile("s_waitcnt lgkmcnt(0)" ::: "memory"); stage_quad(6); }
      if constexpr (s == 0) { TS(22); }
      __builtin_amdgcn_sched_barrier(0);
    });
    TS(23);
    const unsigned pw = lb + rg + r * 272 + h * 16;
#pragma unroll
    for (int m = 0; m < 2; ++m)
#pragma unroll
      for (int n = 0; n < 2; ++n)
#pragma unroll
        for (int g = 0; g < 4; ++g) {
          f32x4 v = {acc[m][n][4 * g], acc[m][n][4 * g + 1], acc[m][n][4 * g + 2], acc[m][n][4 * g + 3]};
          ldsw(pw + m * 32 * 272 + n * 128 + g * 32, v);
        }
  } else {
    if (w == 6) {
      const int ln = min(lane, 13);
      GLB1 unsigned* const fo = (GLB1 unsigned*)(a.ws + OFF_FLG + (c * 14 + ln) * 128 + j * 4);
      GLB1 unsigned* const fi = (GLB1 unsigned*)(a.ws + OFF_FLG + (c * 14 + j) * 128 + ln * 4);
      __hip_atomic_store(fo, 1u, __ATOMIC_RELAXED, __HIP_MEMORY_SCOPE_AGENT);
      for (int spin = 0; spin < SPIN; ++spin) {
        unsigned v = __hip_atomic_load(fi, __ATOMIC_RELAXED, __HIP_MEMORY_SCOPE_AGENT);
        if (__all(v == 1u)) break;
        __builtin_amdgcn_s_sleep(1);
      }
      TS(20);
      if (ACQ) {
        __builtin_amdgcn_fence(__ATOMIC_ACQUIRE, "agent");
        asm volatile("s_waitcnt vmcnt(0)" ::: "memory");
      }
      asm volatile("s_barrier" ::: "memory");
      __hip_atomic_store(fo, 1u, __ATOMIC_RELAXED, __HIP_MEMORY_SCOPE_AGENT);
    } else {
      TS(20);
      asm volatile("s_barrier" ::: "memory");
    }
    TS(21); TS(22); TS(23);
  }
  bar_lgkm();
  TS(24);
#pragma unroll
  for (int e = 0; e < 2; ++e) {
    const int row = (tid >> 4) + 32 * e;
    const unsigned pr = lb + row * 272 + c4 * 16;
    f32x4 sv = ldsr<f32x4>(pr) + ldsr<f32x4>(pr + 24576);
    f32x4 dv = (ldsr<f32x4>(pr + 2 * 24576) + ldsr<f32x4>(pr + 3 * 24576)) + (ldsr<f32x4>(pr + 4 * 24576) + ldsr<f32x4>(pr + 5 * 24576));
    const f32x2 st = ldsr<f32x2>(lb + L_RS + (64 * jg + row) * 8);
    f32x4 o;
#pragma unroll
    for (int i = 0; i < 4; ++i) {
      unsigned xw = xv[e][i >> 1], dw = dk[i >> 1];
      float xf = __uint_as_float((i & 1) ? (xw & 0xffff0000u) : (xw << 16));
      float df = __uint_as_float((i & 1) ? (dw & 0xffff0000u) : (dw << 16));
      o[i] = xf + (2.0f * sv[i] + fmaf(xf, st[0], st[1]) * df) * silu(dv[i]);
    }
    *(f32x4*)(a.out + (size_t)trow[e] * DM + 64 * nt + 4 * c4) = o;
  }
}

extern "C" void kernel_launch(void* const* d_in, const int* in_sizes, int n_in, void* d_out, int out_size,
                              void* d_ws, size_t ws_size, hipStream_t stream) {
  Args a;
  a.x = (const unsigned short*)d_in[0];
  a.lre = (const unsigned short*)d_in[1];
  a.lim = (const unsigned short*)d_in[2];
  a.bre = (const unsigned short*)d_in[3];
  a.bim = (const unsigned short*)d_in[4];
  a.cre = (const unsigned short*)d_in[5];
  a.cim = (const unsigned short*)d_in[6];
  a.ldt = (const unsigned short*)d_in[7];
  a.dsk = (const unsigned short*)d_in[8];
  a.wg = (const unsigned short*)d_in[10];
  a.wu = (const unsigned short*)d_in[11];
  a.wd = (const unsigned short*)d_in[12];
  a.ws = (char*)d_ws;
  a.out = (float*)d_out;
  fused_kernel<<<256, 512, 0, stream>>>(a);
}
```

```cpp
#include <hip/hip_runtime.h>
#include <hip/hip_bf16.h>
#include <stdint.h>
#include <utility>

typedef __attribute__((ext_vector_type(8))) __bf16 bf16x8;
typedef __attribute__((ext_vector_type(4))) __bf16 bf16x4;
typedef __attribute__((ext_vector_type(2))) __bf16 bf16x2;
typedef __attribute__((ext_vector_type(16))) float f32x16;
typedef __attribute__((ext_vector_type(4))) float f32x4;
typedef __attribute__((ext_vector_type(2))) float f32x2;
typedef __attribute__((ext_vector_type(4))) unsigned u32x4;
typedef __attribute__((ext_vector_type(2))) unsigned u32x2;
typedef unsigned long long u64;
#define LDS3 __attribute__((address_space(3)))
#define GLB1 __attribute__((address_space(1)))
#define DEVI __device__ __forceinline__

constexpr int DM = 448, FF = 1792;
constexpr int KS_ALL = 168;
constexpr size_t OFF_AGG = 0;
constexpr size_t OFF_FLG = 224 * 8192;
constexpr size_t OFF_A = 2097152;

template <int... Is, class Fn>
DEVI void static_for_impl(std::integer_sequence<int, Is...>, Fn&& f) {
  (f(std::integral_constant<int, Is>{}), ...);
}
template <int N, class Fn>
DEVI void static_for(Fn&& f) { static_for_impl(std::make_integer_sequence<int, N>{}, f); }

DEVI void glds2(unsigned v0, unsigned v1, const void* sbase, unsigned ldsdst) {
  unsigned keep;
  asm volatile("s_mov_b32 %0, m0\n\ts_mov_b32 m0, %4\n\ts_nop 0\n\tglobal_load_lds_dwordx4 %1, %3\n\t"
               "global_load_lds_dwordx4 %2, %3 offset:1024\n\ts_mov_b32 m0, %0"
               : "=&s"(keep) : "v"(v0), "v"(v1), "s"(sbase), "s"(ldsdst) : "memory");
}
DEVI void glds1(unsigned v0, const void* sbase, unsigned ldsdst) {
  unsigned keep;
  asm volatile("s_mov_b32 %0, m0\n\ts_mov_b32 m0, %3\n\ts_nop 0\n\tglobal_load_lds_dwordx4 %1, %2\n\ts_mov_b32 m0, %0"
               : "=&s"(keep) : "v"(v0), "s"(sbase), "s"(ldsdst) : "memory");
}
DEVI void st16(unsigned vo, const void* sb, u32x4 v) {
  asm volatile("global_store_dwordx4 %0, %1, %2 sc1\n\ts_nop 1" ::"v"(vo), "v"(v), "s"(sb) : "memory");
}
template <int N> DEVI void wait_vm_lgkm0() { asm volatile("s_waitcnt vmcnt(%0) lgkmcnt(0)" ::"n"(N) : "memory"); }
DEVI float bf2f(unsigned short v) { return __uint_as_float((unsigned)v << 16); }
DEVI unsigned pack2(float a, float b) {
  f32x2 p = {a, b};
  return __builtin_bit_cast(unsigned, __builtin_convertvector(p, bf16x2));
}
DEVI float dot2(unsigned a, unsigned b, float c) {
  return __builtin_amdgcn_fdot2_f32_bf16(__builtin_bit_cast(bf16x2, a), __builtin_bit_cast(bf16x2, b), c, false);
}
DEVI bf16x8 lds_b128(unsigned addr) { return *(LDS3 bf16x8*)(uintptr_t)addr; }
DEVI bf16x8 lds_tr(unsigned a0, unsigned a1) {
  bf16x4 a = __builtin_amdgcn_ds_read_tr16_b64_v4bf16((LDS3 bf16x4*)(uintptr_t)a0);
  bf16x4 b = __builtin_amdgcn_ds_read_tr16_b64_v4bf16((LDS3 bf16x4*)(uintptr_t)a1);
  return __builtin_shufflevector(a, b, 0, 1, 2, 3, 4, 5, 6, 7);
}
DEVI f32x16 mfma(bf16x8 a, bf16x8 b, f32x16 c) { return __builtin_amdgcn_mfma_f32_32x32x16_bf16(a, b, c, 0, 0, 0); }
DEVI float silu(float g) { return g * __builtin_amdgcn_rcpf(1.0f + __expf(-g)); }

template <int OFF> DEVI void gld16(u32x4& d, unsigned vo, const void* sb) {
  asm volatile("global_load_dwordx4 %0, %1, %2 offset:%3" : "=v"(d) : "v"(vo), "s"(sb), "n"(OFF) : "memory");
}
#define CODEPF 1
#define CODE_LINES 200
#define ACQ 0
#define WARM2 1
DEVI void gld16_sc1(u32x4& d, unsigned vo, const void* sb) {
  asm volatile("global_load_dwordx4 %0, %1, %2 sc1" : "=v"(d) : "v"(vo), "s"(sb) : "memory");
}
template <int N> DEVI void wait4(u32x4 (&g)[4]) {
  asm volatile("s_waitcnt vmcnt(%4)" : "+v"(g[0]), "+v"(g[1]), "+v"(g[2]), "+v"(g[3]) : "n"(N) : "memory");
}
template <int N> DEVI void wait2(u32x4& x, u32x4& y) {
  asm volatile("s_waitcnt vmcnt(%2)" : "+v"(x), "+v"(y) : "n"(N) : "memory");
}
template <int N> DEVI void wait7(u32x4 (&g)[7]) {
  asm volatile("s_waitcnt vmcnt(%7)" : "+v"(g[0]), "+v"(g[1]), "+v"(g[2]), "+v"(g[3]), "+v"(g[4]), "+v"(g[5]), "+v"(g[6]) : "n"(N) : "memory");
}
DEVI void keepv(float& v) { asm volatile("" : "+v"(v)); }
DEVI void keepi(int& v) { asm volatile("" : "+v"(v)); }
DEVI void useu(unsigned v) { asm volatile("" ::"v"(v)); }
DEVI void gld4(unsigned& d, unsigned vo, const void* sb) { asm volatile("global_load_dword %0, %1, %2" : "=v"(d) : "v"(vo), "s"(sb) : "memory"); }
DEVI void keepu(unsigned& v) { asm volatile("" : "+v"(v)); }
DEVI void cbar() { asm volatile("" ::: "memory"); }
template <int N> DEVI void wait_vm() { asm volatile("s_waitcnt vmcnt(%0)" ::"n"(N) : "memory"); }
DEVI void bar_lgkm() { asm volatile("s_waitcnt lgkmcnt(0)\n\ts_barrier" ::: "memory"); }
template <int N> DEVI void bar_vm() { asm volatile("s_waitcnt vmcnt(%0) lgkmcnt(0)\n\ts_barrier" ::"n"(N) : "memory"); }
template <class T> DEVI T ldsr(unsigned a) { return *(LDS3 T*)(uintptr_t)a; }
template <class T> DEVI void ldsw(unsigned a, T v) { *(LDS3 T*)(uintptr_t)a = v; }
DEVI int tau(int r) { return 16 * ((r >> 2) & 1) + (r & 3) + 4 * (r >> 3); }
template <int C> DEVI float dpp_add(float v) {
  return v + __uint_as_float(__builtin_amdgcn_update_dpp(0u, __float_as_uint(v), C, 0xf, 0xf, true));
}
DEVI void cmul(float& ar, float& ai, float br, float bi) { float t = ar * br - ai * bi; ai = ar * bi + ai * br; ar = t; }
DEVI void csq(float ar, float ai, float& br, float& bi) { br = ar * ar - ai * ai; bi = 2.f * ar * ai; }
DEVI void swap16(float v, float& even, float& odd) {
  auto s = __builtin_amdgcn_permlane16_swap(__float_as_uint(v), __float_as_uint(v), false, false);
  even = __uint_as_float(s[0]); odd = __uint_as_float(s[1]);
}
DEVI void swap32(float v, float& lo, float& hi) {
  auto s = __builtin_amdgcn_permlane32_swap(__float_as_uint(v), __float_as_uint(v), false, false);
  lo = __uint_as_float(s[0]); hi = __uint_as_float(s[1]);
}

#define TS(i)

constexpr int SLOT = 16384;
constexpr int L_D3 = 9 * SLOT;
constexpr int L_SEG = L_D3 + 8192;
constexpr int L_PAR = L_SEG + 1024;
constexpr int L_RS = L_PAR + 512;
constexpr int L_TOT = L_RS + 1024;
constexpr int SPIN = 1 << 18;

struct Args {
  const unsigned short *x, *lre, *lim, *bre, *bim, *cre, *cim, *ldt, *dsk, *wg, *wu, *wd;
  char* ws;
  float* out;
};

constexpr int kstep_of(int s) { return s < 16 ? s : (s < 20 ? s + 8 : (s < 28 ? s - 4 : (s - 28) % 28)); }
constexpr int wslot(int n) { return (n + 1) % 9; }
constexpr int PA = 6;
constexpr int young2(int s) {
  int n = 2 * ((s + PA - 1 < 27 ? s + PA - 1 : 27) - s);
  if (s >= 5 && s <= 4 + PA) n += 8;
  return n;
}

__global__ __launch_bounds__(512, 2) void fused_kernel(Args a) {
  __shared__ __attribute__((aligned(16))) char smem[L_TOT];
  const unsigned lb = (unsigned)(uintptr_t)(LDS3 char*)smem;
  const int xcd = blockIdx.x & 7, idx = blockIdx.x >> 3;
  const int w = __builtin_amdgcn_readfirstlane(threadIdx.x >> 6), lane = threadIdx.x & 63;
  unsigned cpf = 0;
  {
    const char* pc = (const char*)__builtin_amdgcn_s_getpc();
    gld4(cpf, min(w * 64 + lane, CODE_LINES - 1) * 128, pc);
  }
  if (idx >= 28) {
    unsigned acc = 0;
    const int p = (idx - 28) * 8 + w;
#pragma unroll 2
    for (int it = 0; it < 10; ++it) {
      int ln = min((it * 32 + p) * 64 + lane, 18815), qd = ln / 2688, q2 = ln - qd * 2688, kr = q2 / 448, q3 = q2 - kr * 448;
      const unsigned short* src = kr >= 2 ? a.wd + (size_t)(64 * (7 * (kr - 2) + qd)) * DM + q3 * 64
                                          : (kr ? a.cim : a.cre) + (size_t)q3 * DM + qd * 64;
      acc ^= *(const unsigned*)src;
    }
    useu(acc);
    useu(cpf);
    return;
  }
  const int nt = idx % 7, jg = xcd & 1, c = 4 * (xcd >> 1) + idx / 7, j = 7 * jg + nt;
  const int mq = w & 3, nh = w >> 2;
  const int r = lane & 31, h = lane >> 5, o16 = (lane >> 4) & 1, c16 = lane & 15, dist = lane >> 4;
  const char* const wsA = a.ws + OFF_A + (size_t)((4 * c + mq) * KS_ALL) * 1024;
  TS(0);
  unsigned p_re = 0, p_im = 0, p_dt = 0;
  u32x4 raw[2][7];
  if (w == 0) {
    asm volatile("global_load_ushort %0, %1, %2" : "=v"(p_re) : "v"((32 * j + r) * 2), "s"(a.lre) : "memory");
    asm volatile("global_load_ushort %0, %1, %2" : "=v"(p_im) : "v"((32 * j + r) * 2), "s"(a.lim) : "memory");
    asm volatile("global_load_ushort %0, %1, %2" : "=v"(p_dt) : "v"((32 * j + r) * 2), "s"(a.ldt) : "memory");
  }
  int rhog[2];
  const char* const xb = (const char*)a.x + (size_t)(128 * c) * 896;
#pragma unroll
  for (int g = 0; g < 2; ++g) {
    const int rho = 8 * ((8 * nh + 4 * g + mq + 2 * nt + 1) & 15) + (lane >> 3);
    rhog[g] = rho;
    const unsigned vo = ((rho & 96) + tau(rho & 31)) * 896 + (lane & 7) * 16;
    static_for<7>([&](auto I) { gld16<I * 128>(raw[g][I], vo, xb); });
  }
  const unsigned vq = (lane >> 3) * 896 + (((lane & 7) ^ ((4 * w + (lane >> 4)) & 7)) << 4);
  const char* const bb = (const char*)((w & 2) ? a.bim : a.bre) + (32 * j + 8 * (w & 1)) * 896;
  const char* const wb = (const char*)(nh ? a.wu : a.wg) + 256 * j;
  const char* const inbox = a.ws + OFF_AGG + (size_t)(c * 14 + j) * 8192;
  unsigned warm = 0, warm2 = 0;
  if (nh == 1) {
#pragma unroll
    for (int i = 0; i < 5; ++i) {
      const int slab = i < 4 ? i : 6;
      glds1(vq, bb + slab * 128, lb + 7 * SLOT + i * 8192 + mq * 1024);
      glds1(vq, bb + slab * 128 + 16 * 896, lb + 7 * SLOT + i * 8192 + mq * 1024 + 4096);
    }
  }
  TS(12);
#pragma unroll
  for (int g = 0; g < 2; ++g) {
    if (nh == 0) { if (g == 0) wait_vm<7>(); else wait_vm<0>(); }
    if (g == 0) wait7<17>(raw[0]); else wait7<10>(raw[1]);
    if (g == 0) { TS(1); } else { TS(14); }
    float s1a = 0.f, s2a = 0.f, s1b = 0.f, s2b = 0.f;
#pragma unroll
    for (int i = 0; i < 7; ++i) {
      s1a = dot2(raw[g][i][0], 0x3f803f80u, s1a); s2a = dot2(raw[g][i][0], raw[g][i][0], s2a);
      s1b = dot2(raw[g][i][1], 0x3f803f80u, s1b); s2b = dot2(raw[g][i][1], raw[g][i][1], s2b);
      s1a = dot2(raw[g][i][2], 0x3f803f80u, s1a); s2a = dot2(raw[g][i][2], raw[g][i][2], s2a);
      s1b = dot2(raw[g][i][3], 0x3f803f80u, s1b); s2b = dot2(raw[g][i][3], raw[g][i][3], s2b);
    }
    float s1 = s1a + s1b, s2 = s2a + s2b;
    s1 = dpp_add<0xB1>(s1); s2 = dpp_add<0xB1>(s2);
    s1 = dpp_add<0x4E>(s1); s2 = dpp_add<0x4E>(s2);
    s1 = dpp_add<0x141>(s1); s2 = dpp_add<0x141>(s2);
    const float mean = s1 * (1.0f / DM);
    const float rstd = __builtin_amdgcn_rsqf(fmaxf(s2 * (1.0f / DM) - mean * mean, 0.f) + 1e-5f);
    const float nb = -mean * rstd;
    const int rho = rhog[g];
    const unsigned wa = lb + rho * 128 + (((lane & 7) ^ ((rho >> 1) & 7)) << 4);
#pragma unroll
    for (int i = 0; i < 7; ++i) {
      u32x4 ov;
#pragma unroll
      for (int e = 0; e < 4; ++e) {
        unsigned u = raw[g][i][e];
        ov[e] = pack2(fmaf(__uint_as_float(u << 16), rstd, nb), fmaf(__uint_as_float(u & 0xffff0000u), rstd, nb));
      }
      ldsw(wa + i * SLOT, ov);
    }
    ldsw(lb + L_RS + rho * 8, f32x2{rstd, nb});
  }
  useu(cpf);
  TS(2);
  {
    int ln = min(mq * 64 + lane, 223) * 4 + (c & 3), un = ln >= 448, row = ln - 448 * un;
    asm volatile("global_load_dword %0, %1, %2" : "=v"(warm) : "v"(row * 3584 + un * 128), "s"(wb) : "memory");
  }
  if (w == 0) {
    asm volatile("" : "+v"(p_re), "+v"(p_im), "+v"(p_dt));
    float dt = expf(bf2f(p_dt)), are = bf2f(p_re), aim = bf2f(p_im);
    float e = expf(dt * are), sn, cn;
    sincosf(dt * aim, &sn, &cn);
    float lr = e * cn, li = e * sn;
    float inv = 1.0f / (are * are + aim * aim);
    float nr = lr - 1.0f, ni = li;
    f32x4 pv = {lr, li, (nr * are + ni * aim) * inv, (ni * are - nr * aim) * inv};
    ldsw(lb + L_PAR + r * 16, pv);
  }
  TS(13);
  auto stage_b = [&](int slab, unsigned dst) { glds1(vq, bb + slab * 128 + nh * (16 * 896), lb + dst + w * 1024); };
  const unsigned vw0 = (16 * mq + (lane >> 3)) * 3584 + (((lane & 7) ^ (2 * (2 * nh + ((lane >> 4) & 1)))) << 4);
  const unsigned vw1 = vw0 + 8 * 3584 - 1024;
  auto stage_w = [&](int n) {
    glds2(vw0, vw1, wb + (n / 7) * 128 + (n % 7) * (64 * 3584), lb + wslot(n) * SLOT + w * 2048);
  };
  auto quad_src = [&](int kr, int qd) {
    return kr >= 2 ? (const char*)a.wd + (size_t)(64 * (7 * (kr - 2) + qd)) * 896 + nt * 128
                   : (const char*)(kr ? a.cim : a.cre) + (size_t)(64 * nt) * 896 + qd * 128;
  };
  auto stage_p = [&](int sl) {
#pragma unroll
    for (int e = 0; e < 2; ++e) {
      const int u = 2 * sl + e;
      glds1(vq, quad_src(u / 3, u % 3) + w * (8 * 896), lb + u * 8192 + w * 1024);
    }
  };

  unsigned toff[4];
#pragma unroll
  for (int ks = 0; ks < 4; ++ks) toff[ks] = lb + r * 128 + (((2 * ks + h) ^ ((r >> 1) & 7)) << 4);
  const int q4 = (lane >> 2) & 3;
  unsigned wta[2][3];
#pragma unroll
  for (int t = 0; t < 2; ++t) {
    wta[t][0] = lb + o16 * 8192 + (8 * h + q4) * 128 + (((2 * nh + t) ^ (2 * o16 + (q4 >> 1))) << 5) + (lane & 3) * 8;
    wta[t][1] = wta[t][0] + 3 * SLOT; wta[t][2] = wta[t][0] + 6 * SLOT;
    keepu(wta[t][1]); keepu(wta[t][2]);
  }

  {
  bf16x8 xf[28];
  bf16x8 fs[4], fr[3][2];
  f32x16 accs = {}, accu[2][2] = {};
  float lr, li, cfr, cfi, l64r, l64i, lcr, lci, l2r, l2i, l4r, l4i, yr = 0.f, yi = 0.f, t0r, t0i, t1r, t1i;
  float l8r, l8i, l16r, l16i, l32r, l32i, clr = 0.f, cli = 0.f, er = 0.f, ei = 0.f;
  float ur[8], ui[8];
  u32x4 gr[4], ov[2];
  float pbr, pbi;
  f32x2 sg[4];
  const char* const pubb = a.ws + OFF_AGG + (4 * mq * 14 + j) * 8192 + c * 512 + nh * 256;
  auto publish = [&]() {
    int l2 = lane; keepi(l2);
    st16((l2 >> 4) * (14 * 8192) + (l2 & 15) * 16, pubb, u32x4{__float_as_uint(pbr), 1u, __float_as_uint(pbi), 1u});
  };

  auto load_x = [&](auto KI) {
    constexpr int k = KI;
    xf[k] = lds_b128(toff[k & 3] + mq * 4096 + (k >> 2) * SLOT);
  };
  auto load_s = [&](auto SI) {
    constexpr int s = SI, k = kstep_of(s), sl = k >> 2;
    constexpr int off = sl < 4 ? 7 * SLOT + sl * 8192 : (sl == 6 ? L_D3 : (sl - 4) * 8192);
    fs[s & 3] = lds_b128(toff[k & 3] + nh * 4096 + off);
  };
  auto load_w = [&](auto SI, int t) {
    constexpr int s = SI;
    if constexpr (s < 84) {
      constexpr int n = (s - 28) >> 2, ks = s & 3, sl = wslot(n);
      unsigned ad = wta[t][sl / 3] + (sl % 3) * SLOT + ks * 2048;
      fr[s % 3][t] = lds_tr(ad, ad + 512);
    }
  };
  auto poll = [&]() {
    int l2 = lane; keepi(l2);
#pragma unroll
    for (int n = 0; n < 4; ++n) gld16_sc1(gr[n], ((c - 1 - 4 * n - (l2 >> 4)) & 15) * 512 + (l2 & 15) * 16, inbox + nh * 256);
  };
  auto act1 = [&](float g, float u) { return (g * u) * __builtin_amdgcn_rcpf(1.0f + __builtin_amdgcn_exp2f(g * -1.44269504f)); };
  auto epi2 = [&](const f32x16& ac, int i) { return pack2(act1(ac[2 * i], ac[8 + 2 * i]), act1(ac[2 * i + 1], ac[9 + 2 * i])); };

  auto hook = [&](auto QI) {
    constexpr int q = QI;
    if constexpr (q >= 2 && q < 18) {
      constexpr int i = (q - 2) >> 1;
      if constexpr ((q & 1) == 0) {
        auto sw = __builtin_amdgcn_permlane16_swap(__float_as_uint(accs[i]), __float_as_uint(accs[i + 8]), false, false);
        float vr = __uint_as_float(sw[0]), vi = __uint_as_float(sw[1]);
        ur[i] = cfr * vr - cfi * vi; ui[i] = cfr * vi + cfi * vr;
      } else {
        float t = lr * er - li * ei + ur[i];
        ei = lr * ei + li * er + ui[i]; er = t;
      }
    }
    if constexpr (q == 13) stage_w(9);
    if constexpr (q == 18) { swap16(er, t0r, yr); swap16(ei, t0i, yi); }
    if constexpr (q == 19) { er = l8r * t0r - l8i * t0i + yr; ei = l8r * t0i + l8i * t0r + yi; }
    if constexpr (q == 20) { swap32(er, t1r, yr); swap32(ei, t1i, yi); }
    if constexpr (q == 21) {
      ldsw(lb + L_SEG + ((nh * 4 + mq) * 16 + c16) * 8, f32x2{l16r * t1r - l16i * t1i + yr, l16r * t1i + l16i * t1r + yi});
    }
    if constexpr (q == 22) stage_w(10);
    if constexpr (q == 23) { t1r = h ? t1r : 0.f; t1i = h ? t1i : 0.f; yr = l8r * t1r - l8i * t1i + t0r; yi = l8r * t1i + l8i * t1r + t0i; }
    if constexpr (q == 24) { clr = o16 ? yr : t1r; cli = o16 ? yi : t1i; csq(l32r, l32i, l64r, l64i); }
    if constexpr (q == 25) { yr = h ? l16r : 1.f; yi = h ? l16i : 0.f; t1r = o16 ? l8r : 1.f; t1i = o16 ? l8i : 0.f; }
    if constexpr (q == 26) cmul(yr, yi, t1r, t1i);
    if constexpr (q == 28) {
#pragma unroll
      for (int i = 0; i < 4; ++i) sg[i] = ldsr<f32x2>(lb + L_SEG + ((nh * 4 + i) * 16 + c16) * 8);
    }
    if constexpr (q == 29) stage_w(11);
    if constexpr (q == 30) { t0r = l32r * sg[0][0] - l32i * sg[0][1] + sg[1][0]; t0i = l32r * sg[0][1] + l32i * sg[0][0] + sg[1][1]; }
    if constexpr (q == 31) { t1r = l32r * t0r - l32i * t0i + sg[2][0]; t1i = l32r * t0i + l32i * t0r + sg[2][1]; }
    if constexpr (q == 32) { pbr = l32r * t1r - l32i * t1i + sg[3][0]; pbi = l32r * t1i + l32i * t1r + sg[3][1]; }
    if constexpr (q == 33) publish();
    if constexpr (q == 34) {
      er = mq == 0 ? 0.f : (mq == 1 ? sg[0][0] : (mq == 2 ? t0r : t1r));
      ei = mq == 0 ? 0.f : (mq == 1 ? sg[0][1] : (mq == 2 ? t0i : t1i));
    }
    if constexpr (q == 35) { clr += yr * er - yi * ei; cli += yr * ei + yi * er; }
    if constexpr (q == 36) { t1r = (mq & 1) ? l32r : 1.f; t1i = (mq & 1) ? l32i : 0.f; cmul(yr, yi, t1r, t1i); }
    if constexpr (q == 37) { t0r = (mq & 2) ? l64r : 1.f; t0i = (mq & 2) ? l64i : 0.f; cmul(t0r, t0i, yr, yi); }
    if constexpr (q == 38) stage_w(12);
    if constexpr (q == 45) stage_w(13);
    if constexpr (q >= 58 && q < 78 && q != 61 && q != 66 && q != 70 && q != 74) {
      constexpr int e = q - 58 - (q > 61) - (q > 66) - (q > 70) - (q > 74), t = e >> 3, i = e & 7;
      if constexpr ((i & 1) == 0) t1r = act1(accu[0][t][i], accu[0][t][8 + i]);
      else ov[t][i >> 1] = pack2(t1r, act1(accu[0][t][i], accu[0][t][8 + i]));
      if constexpr (i == 7) st16(lane * 16, wsA + (56 + 8 * j + 2 * nh + t) * 1024, ov[t]);
    }
    if constexpr (q == 61) stage_p(7);
    if constexpr (q == 66) stage_p(8);
    if constexpr (q == 70) poll();
    if constexpr (q == 74) { csq(l64r, l64i, lcr, lci); csq(lcr, lci, l2r, l2i); }
    if constexpr (q == 78) stage_p(0);
    if constexpr (q == 79) csq(l2r, l2i, l4r, l4i);
    if constexpr (q == 80) {
      wait4<3>(gr);
      for (int spin = 0; spin < SPIN; ++spin) {
        unsigned ok = (gr[0][1] & gr[0][3] & gr[1][1]) & (gr[1][3] & gr[2][1] & gr[2][3]) & (gr[3][1] & gr[3][3]);
        if (__all(ok != 0u)) break;
        __builtin_amdgcn_s_sleep(2);
        poll();
        wait4<0>(gr);
      }
      TS(29);
    }
    if constexpr (q == 81) { keepv(pbr); publish(); }
    if constexpr (q >= 82 && q < 86) {
      constexpr int n = 85 - q;
      bool v = 4 * n + dist < c;
      float xr = v ? __uint_as_float(gr[n][0]) : 0.f, xi = v ? __uint_as_float(gr[n][2]) : 0.f;
      if constexpr (n == 3) { yr = xr; yi = xi; }
      else { float t = l4r * yr - l4i * yi + xr; yi = l4r * yi + l4i * yr + xi; yr = t; }
    }
    if constexpr (q == 86) stage_p(1);
    if constexpr (q == 87) { swap16(yr, er, t1r); swap16(yi, ei, t1i); }
    if constexpr (q == 88) { yr = er + lcr * t1r - lci * t1i; yi = ei + lcr * t1i + lci * t1r; }
    if constexpr (q == 89) { swap32(yr, er, t1r); swap32(yi, ei, t1i); }
    if constexpr (q == 90) { yr = er + l2r * t1r - l2i * t1i; yi = ei + l2r * t1i + l2i * t1r; }
    if constexpr (q == 91) { er = clr + t0r * yr - t0i * yi; ei = cli + t0r * yi + t0i * yr; }
    if constexpr (q == 92) stage_p(2);
    if constexpr (q >= 93 && q < 101) {
      constexpr int i = q - 93;
      float t = lr * er - li * ei + ur[i];
      ei = lr * ei + li * er + ui[i]; er = t;
      ldsw(lb + 6 * SLOT + w * 2048 + ((i & 3) + 8 * (i >> 2) + 16 * o16 + 4 * h) * 64 + ((c16 * 4) ^ (((2 * (i >> 2) + h) & 3) << 4)), pack2(er, -ei));
    }
    if constexpr (q == 101) {
      cbar();
#pragma unroll
      for (int e = 0; e < 2; ++e) ov[e] = ldsr<u32x4>(lb + 6 * SLOT + w * 2048 + r * 64 + (((2 * h + e) ^ ((r >> 2) & 3)) << 4));
    }
    if constexpr (q == 102) stage_p(3);
    if constexpr (q == 104 || q == 105) {
      u32x4 o;
#pragma unroll
      for (int i = 0; i < 4; ++i)
        o[i] = __builtin_amdgcn_perm(ov[i >> 1][2 * (i & 1) + 1], ov[i >> 1][2 * (i & 1)], q == 104 ? 0x05040100u : 0x07060302u);
      st16(lane * 16, wsA + ((q - 104) * 28 + 2 * j + nh) * 1024, o);
    }
    if constexpr (q >= 108 && q < 111) stage_p(q - 104);
  };

  bar_vm<1>();
  TS(3);
  static_for<4>(load_x);
  static_for<3>(load_s);

  static_for<84>([&](auto SI) {
    constexpr int s = SI;
    if constexpr (s == 3 || s == 6) { bar_lgkm(); TS(s == 3 ? 4 : 5); }
    if constexpr (s == 17) { bar_vm<10>(); useu(warm); TS(6); }
    if constexpr (s == 26) { bar_vm<14>(); TS(7); }
    if constexpr (s == 34) { bar_vm<10>(); TS(8); }
    if constexpr (s == 42) { bar_vm<10>(); TS(9); }
    if constexpr (s == 50) { bar_vm<11>(); TS(10); }
    if constexpr (s == 58) { bar_vm<0>(); TS(11); }
    if constexpr (s == 66 || s == 74 || s == 82) { bar_lgkm(); TS(26 + (s - 66) / 8); }
    if constexpr (s < 28) {
      if constexpr (s < 6) static_for<4>([&](auto I) { load_x(std::integral_constant<int, 4 * (s + 1) + I>{}); });
      if constexpr (s + 3 < 28) load_s(std::integral_constant<int, s + 3>{});
      if constexpr (s >= 26) { load_w(std::integral_constant<int, s + 2>{}, 0); load_w(std::integral_constant<int, s + 2>{}, 1); }
      __builtin_amdgcn_sched_barrier(0);
      accs = mfma(xf[kstep_of(s)], fs[s & 3], accs);
      __builtin_amdgcn_sched_barrier(0);
      if constexpr (s == 3) { stage_b(4, 0); stage_b(5, 8192); }
      if constexpr (s == 5) stage_w(0);
      if constexpr (s == 8) stage_w(1);
      if constexpr (s == 11) stage_w(2);
      if constexpr (s == 13) {
        u32x4 z = {0u, 0u, 0u, 0u};
        st16(w == 6 && lane < 8 ? (unsigned)(OFF_FLG - OFF_AGG) + lane * 16 - (c * 14 + j) * 8064 : w * 1024 + lane * 16, inbox, z);
        st16(w * 1024 + lane * 16, inbox, z);
      }
      if constexpr (s == 15) stage_w(3);
      if constexpr (s == 18 || s == 20 || s == 22 || s == 24) stage_w(4 + (s - 18) / 2);
      if constexpr (s == 27) stage_w(8);
      if constexpr (s == 16) {
        f32x4 pv = ldsr<f32x4>(lb + L_PAR + (16 * nh + c16) * 16);
        lr = pv[0]; li = pv[1]; cfr = pv[2]; cfi = pv[3];
      }
      if constexpr (s == 19) { csq(lr, li, t0r, t0i); csq(t0r, t0i, t1r, t1i); }
      if constexpr (s == 21) { csq(t1r, t1i, l8r, l8i); csq(l8r, l8i, l16r, l16i); }
      if constexpr (s == 23) csq(l16r, l16i, l32r, l32i);
      __builtin_amdgcn_sched_barrier(0);
    } else {
      constexpr int u = (s - 28) / 28;
      accu[u][0] = mfma(fr[s % 3][0], xf[kstep_of(s)], accu[u][0]);
      __builtin_amdgcn_sched_barrier(0);
      load_w(std::integral_constant<int, s + 2>{}, 0);
      hook(std::integral_constant<int, 2 * (s - 28)>{});
      __builtin_amdgcn_sched_barrier(0);
      accu[u][1] = mfma(fr[s % 3][1], xf[kstep_of(s)], accu[u][1]);
      __builtin_amdgcn_sched_barrier(0);
      load_w(std::integral_constant<int, s + 2>{}, 1);
      hook(std::integral_constant<int, 2 * (s - 28) + 1>{});
      __builtin_amdgcn_sched_barrier(0);
    }
  });
  TS(15);
#pragma unroll
  for (int t = 0; t < 2; ++t) {
#pragma unroll
    for (int i = 0; i < 4; ++i) ov[t][i] = epi2(accu[1][t], i);
    st16(lane * 16, wsA + (56 + 8 * j + 4 + 2 * nh + t) * 1024, ov[t]);
  }
  }
  TS(16);
  bar_vm<0>();
  TS(17);

  int tid = threadIdx.x;
  asm volatile("" : "+v"(tid));
  const int c4 = tid & 15;
  u32x2 xv[2];
  const u32x2 dk = *(const u32x2*)(a.dsk + 64 * nt + 4 * c4);
  int trow[2];
#pragma unroll
  for (int e = 0; e < 2; ++e) {
    trow[e] = 128 * c + 64 * jg + 32 * e + tau(tid >> 4);
    xv[e] = *(const u32x2*)(a.x + (size_t)trow[e] * DM + 64 * nt + 4 * c4);
  }
  if (w < 6) {
    const int lane = tid & 63, r = lane & 31, h = lane >> 5, o16 = (lane >> 4) & 1, q4 = (lane >> 2) & 3;
    unsigned toff[4];
#pragma unroll
    for (int ks = 0; ks < 4; ++ks) toff[ks] = lb + r * 128 + (((2 * ks + h) ^ ((r >> 1) & 7)) << 4);
    bf16x8 bq[3][4][2];
    u32x4 af[PA][2];
    f32x16 acc[2][2] = {};
    const unsigned rg = w * 24576;
    unsigned tra[2][2];
#pragma unroll
    for (int n = 0; n < 2; ++n)
#pragma unroll
      for (int e = 0; e < 2; ++e)
        tra[n][e] = lb + rg + (4 * h + q4 + 8 * e) * 128 + (((4 * n + 2 * o16 + ((lane >> 1) & 1)) ^ ((2 * h + (q4 >> 1) + 4 * e) & 7)) << 4) + (lane & 1) * 8;
    auto read_quad = [&](int u) {
      if (w < 2) {
#pragma unroll
        for (int i = 0; i < 4; ++i)
#pragma unroll
          for (int n = 0; n < 2; ++n) bq[u][i][n] = lds_b128(toff[i] + rg + u * 8192 + n * 4096);
      } else {
#pragma unroll
        for (int i = 0; i < 4; ++i)
#pragma unroll
          for (int n = 0; n < 2; ++n) bq[u][i][n] = lds_tr(tra[n][0] + u * 8192 + i * 2048, tra[n][1] + u * 8192 + i * 2048);
      }
    };
    const unsigned vq0 = (lane >> 3) * 896 + (((lane & 7) ^ (lane >> 4)) << 4);
    const unsigned vq1 = ((lane >> 3) + 8) * 896 + (((lane & 7) ^ (4 + (lane >> 4))) << 4) - 1024;
    auto stage_quad = [&](int qd) {
      const char* src = quad_src(w, qd);
#pragma unroll
      for (int i = 0; i < 4; ++i) glds2(vq0, vq1, src + i * (16 * 896), lb + rg + (qd % 3) * 8192 + i * 2048);
    };
    read_quad(0); read_quad(1); read_quad(2);
    asm volatile("s_waitcnt lgkmcnt(0)" ::: "memory");
    __builtin_amdgcn_sched_barrier(0);
    stage_quad(3); stage_quad(4); stage_quad(5);
    TS(20);
    __builtin_amdgcn_s_barrier();
    TS(21);
    const char* const pa = a.ws + OFF_A + (size_t)((4 * c + 2 * jg) * KS_ALL + 28 * w) * 1024;
    const unsigned l0 = lane * 16, l1 = lane * 16 + KS_ALL * 1024;
    auto lda = [&](int s) {
      if (ACQ) { gld16<0>(af[s % PA][0], l0, pa + s * 1024); gld16<0>(af[s % PA][1], l1, pa + s * 1024); }
      else { gld16_sc1(af[s % PA][0], l0, pa + s * 1024); gld16_sc1(af[s % PA][1], l1, pa + s * 1024); }
    };
#pragma unroll
    for (int s = 0; s < PA; ++s) lda(s);
    static_for<28>([&](auto SI) {
      constexpr int s = SI;
      wait2<young2(s)>(af[s % PA][0], af[s % PA][1]);
#pragma unroll
      for (int m = 0; m < 2; ++m)
#pragma unroll
        for (int n = 0; n < 2; ++n)
          acc[m][n] = mfma(bq[(s >> 2) % 3][s & 3][n], __builtin_bit_cast(bf16x8, af[s % PA][m]), acc[m][n]);
      __builtin_amdgcn_sched_barrier(0);
      if constexpr (s + PA < 28) lda(s + PA);
      if constexpr ((s & 3) == 3 && s < 16) read_quad((s >> 2) % 3);
      if constexpr (s == 4) { asm volatile("s_waitcnt lgkmcnt(0)" ::: "memory"); stage_quad(6); }
      if constexpr (s == 0) { TS(22); }
      __builtin_amdgcn_sched_barrier(0);
    });
    TS(23);
    const unsigned pw = lb + rg + r * 272 + h * 16;
#pragma unroll
    for (int m = 0; m < 2; ++m)
#pragma unroll
      for (int n = 0; n < 2; ++n)
#pragma unroll
        for (int g = 0; g < 4; ++g) {
          f32x4 v = {acc[m][n][4 * g], acc[m][n][4 * g + 1], acc[m][n][4 * g + 2], acc[m][n][4 * g + 3]};
          ldsw(pw + m * 32 * 272 + n * 128 + g * 32, v);
        }
  } else {
    if (w == 6) {
      const int ln = min(lane, 13);
      GLB1 unsigned* const fo = (GLB1 unsigned*)(a.ws + OFF_FLG + (c * 14 + ln) * 128 + j * 4);
      GLB1 unsigned* const fi = (GLB1 unsigned*)(a.ws + OFF_FLG + (c * 14 + j) * 128 + ln * 4);
      __hip_atomic_store(fo, 1u, __ATOMIC_RELAXED, __HIP_MEMORY_SCOPE_AGENT);
      for (int spin = 0; spin < SPIN; ++spin) {
        unsigned v = __hip_atomic_load(fi, __ATOMIC_RELAXED, __HIP_MEMORY_SCOPE_AGENT);
        if (__all(v == 1u)) break;
        __builtin_amdgcn_s_sleep(1);
      }
      TS(20);
      if (ACQ) {
        __builtin_amdgcn_fence(__ATOMIC_ACQUIRE, "agent");
        asm volatile("s_waitcnt vmcnt(0)" ::: "memory");
      }
      asm volatile("s_barrier" ::: "memory");
      __hip_atomic_store(fo, 1u, __ATOMIC_RELAXED, __HIP_MEMORY_SCOPE_AGENT);
    } else {
      TS(20);
      asm volatile("s_barrier" ::: "memory");
    }
    TS(21); TS(22); TS(23);
  }
  bar_lgkm();
  TS(24);
#pragma unroll
  for (int e = 0; e < 2; ++e) {
    const int row = (tid >> 4) + 32 * e;
    const unsigned pr = lb + row * 272 + c4 * 16;
    f32x4 sv = ldsr<f32x4>(pr) + ldsr<f32x4>(pr + 24576);
    f32x4 dv = (ldsr<f32x4>(pr + 2 * 24576) + ldsr<f32x4>(pr + 3 * 24576)) + (ldsr<f32x4>(pr + 4 * 24576) + ldsr<f32x4>(pr + 5 * 24576));
    const f32x2 st = ldsr<f32x2>(lb + L_RS + (64 * jg + row) * 8);
    f32x4 o;
#pragma unroll
    for (int i = 0; i < 4; ++i) {
      unsigned xw = xv[e][i >> 1], dw = dk[i >> 1];
      float xf = __uint_as_float((i & 1) ? (xw & 0xffff0000u) : (xw << 16));
      float df = __uint_as_float((i & 1) ? (dw & 0xffff0000u) : (dw << 16));
      o[i] = xf + (2.0f * sv[i] + fmaf(xf, st[0], st[1]) * df) * silu(dv[i]);
    }
    *(f32x4*)(a.out + (size_t)trow[e] * DM + 64 * nt + 4 * c4) = o;
  }
}

extern "C" void kernel_launch(void* const* d_in, const int* in_sizes, int n_in, void* d_out, int out_size,
                              void* d_ws, size_t ws_size, hipStream_t stream) {
  Args a;
  a.x = (const unsigned short*)d_in[0];
  a.lre = (const unsigned short*)d_in[1];
  a.lim = (const unsigned short*)d_in[2];
  a.bre = (const unsigned short*)d_in[3];
  a.bim = (const unsigned short*)d_in[4];
  a.cre = (const unsigned short*)d_in[5];
  a.cim = (const unsigned short*)d_in[6];
  a.ldt = (const unsigned short*)d_in[7];
  a.dsk = (const unsigned short*)d_in[8];
  a.wg = (const unsigned short*)d_in[10];
  a.wu = (const unsigned short*)d_in[11];
  a.wd = (const unsigned short*)d_in[12];
  a.ws = (char*)d_ws;
  a.out = (float*)d_out;
  fused_kernel<<<256, 512, 0, stream>>>(a);
}
```

```cpp
#include <hip/hip_runtime.h>
#include <hip/hip_bf16.h>
#include <stdint.h>
#include <utility>

typedef __attribute__((ext_vector_type(8))) __bf16 bf16x8;
typedef __attribute__((ext_vector_type(4))) __bf16 bf16x4;
typedef __attribute__((ext_vector_type(2))) __bf16 bf16x2;
typedef __attribute__((ext_vector_type(16))) float f32x16;
typedef __attribute__((ext_vector_type(4))) float f32x4;
typedef __attribute__((ext_vector_type(2))) float f32x2;
typedef __attribute__((ext_vector_type(4))) unsigned u32x4;
typedef __attribute__((ext_vector_type(2))) unsigned u32x2;
typedef unsigned long long u64;
#define LDS3 __attribute__((address_space(3)))
#define GLB1 __attribute__((address_space(1)))
#define DEVI __device__ __forceinline__

constexpr int DM = 448, FF = 1792;
constexpr int KS_ALL = 168;
constexpr size_t OFF_AGG = 0;
constexpr size_t OFF_FLG = 224 * 8192;
constexpr size_t OFF_A = 2097152;

template <int... Is, class Fn>
DEVI void static_for_impl(std::integer_sequence<int, Is...>, Fn&& f) {
  (f(std::integral_constant<int, Is>{}), ...);
}
template <int N, class Fn>
DEVI void static_for(Fn&& f) { static_for_impl(std::make_integer_sequence<int, N>{}, f); }

template <int OFF> DEVI void glds2(unsigned v0, unsigned v1, const void* sbase, unsigned base) {
  asm volatile("s_add_i32 m0, %3, %4\n\ts_nop 0\n\tglobal_load_lds_dwordx4 %0, %2\n\tglobal_load_lds_dwordx4 %1, %2 offset:1024"
               ::"v"(v0), "v"(v1), "s"(sbase), "s"(base), "n"(OFF) : "memory", "scc");
}
template <int OFF> DEVI void glds1(unsigned v0, const void* sbase, unsigned base) {
  asm volatile("s_add_i32 m0, %2, %3\n\ts_nop 0\n\tglobal_load_lds_dwordx4 %0, %1"
               ::"v"(v0), "s"(sbase), "s"(base), "n"(OFF) : "memory", "scc");
}
#define IC(n) std::integral_constant<int, (n)>{}
DEVI void cfma(float& o_r, float& o_i, float ar, float ai, float br, float bi, float cr, float ci) {
  float x, y;
  asm volatile("v_fma_f32 %0, %2, %4, %6\n\tv_fma_f32 %1, %2, %5, %7\n\tv_fma_f32 %0, -%3, %5, %0\n\tv_fma_f32 %1, %3, %4, %1"
               : "=&v"(x), "=&v"(y) : "v"(ar), "v"(ai), "v"(br), "v"(bi), "v"(cr), "v"(ci));
  o_r = x; o_i = y;
}
DEVI void cmul(float& o_r, float& o_i, float ar, float ai, float br, float bi) {
  float x, y;
  asm volatile("v_mul_f32 %0, %2, %4\n\tv_mul_f32 %1, %2, %5\n\tv_fma_f32 %0, -%3, %5, %0\n\tv_fma_f32 %1, %3, %4, %1"
               : "=&v"(x), "=&v"(y) : "v"(ar), "v"(ai), "v"(br), "v"(bi));
  o_r = x; o_i = y;
}
DEVI void csq(float ar, float ai, float& br, float& bi) { cmul(br, bi, ar, ai, ar, ai); }
DEVI void pin2(float& x, float& y) { asm volatile("" : "+v"(x), "+v"(y)); }
DEVI void st16(unsigned vo, const void* sb, u32x4 v) {
  asm volatile("global_store_dwordx4 %0, %1, %2 sc1\n\ts_nop 1" ::"v"(vo), "v"(v), "s"(sb) : "memory");
}
template <int N> DEVI void wait_vm_lgkm0() { asm volatile("s_waitcnt vmcnt(%0) lgkmcnt(0)" ::"n"(N) : "memory"); }
DEVI float bf2f(unsigned short v) { return __uint_as_float((unsigned)v << 16); }
DEVI unsigned pack2(float a, float b) {
  f32x2 p = {a, b};
  return __builtin_bit_cast(unsigned, __builtin_convertvector(p, bf16x2));
}
DEVI float dot2(unsigned a, unsigned b, float c) {
  return __builtin_amdgcn_fdot2_f32_bf16(__builtin_bit_cast(bf16x2, a), __builtin_bit_cast(bf16x2, b), c, false);
}
DEVI bf16x8 lds_b128(unsigned addr) { return *(LDS3 bf16x8*)(uintptr_t)addr; }
DEVI bf16x8 lds_tr(unsigned a0, unsigned a1) {
  bf16x4 a = __builtin_amdgcn_ds_read_tr16_b64_v4bf16((LDS3 bf16x4*)(uintptr_t)a0);
  bf16x4 b = __builtin_amdgcn_ds_read_tr16_b64_v4bf16((LDS3 bf16x4*)(uintptr_t)a1);
  return __builtin_shufflevector(a, b, 0, 1, 2, 3, 4, 5, 6, 7);
}
DEVI f32x16 mfma(bf16x8 a, bf16x8 b, f32x16 c) { return __builtin_amdgcn_mfma_f32_32x32x16_bf16(a, b, c, 0, 0, 0); }
DEVI float silu(float g) { return g * __builtin_amdgcn_rcpf(1.0f + __expf(-g)); }

template <int OFF> DEVI void gld16(u32x4& d, unsigned vo, const void* sb) {
  asm volatile("global_load_dwordx4 %0, %1, %2 offset:%3" : "=v"(d) : "v"(vo), "s"(sb), "n"(OFF) : "memory");
}
#define CODEPF 1
#define CODE_LINES 200
#define STAG 0
#define ACQ 0
#define WARM2 1
DEVI void gld16_sc1(u32x4& d, unsigned vo, const void* sb) {
  asm volatile("global_load_dwordx4 %0, %1, %2 sc1" : "=v"(d) : "v"(vo), "s"(sb) : "memory");
}
template <int N> DEVI void wait4(u32x4 (&g)[4]) {
  asm volatile("s_waitcnt vmcnt(%4)" : "+v"(g[0]), "+v"(g[1]), "+v"(g[2]), "+v"(g[3]) : "n"(N) : "memory");
}
template <int N> DEVI void wait2(u32x4& x, u32x4& y) {
  asm volatile("s_waitcnt vmcnt(%2)" : "+v"(x), "+v"(y) : "n"(N) : "memory");
}
template <int N> DEVI void wait7(u32x4 (&g)[7]) {
  asm volatile("s_waitcnt vmcnt(%7)" : "+v"(g[0]), "+v"(g[1]), "+v"(g[2]), "+v"(g[3]), "+v"(g[4]), "+v"(g[5]), "+v"(g[6]) : "n"(N) : "memory");
}
DEVI void keepv(float& v) { asm volatile("" : "+v"(v)); }
DEVI void keepi(int& v) { asm volatile("" : "+v"(v)); }
DEVI void useu(unsigned v) { asm volatile("" ::"v"(v)); }
DEVI void gld4(unsigned& d, unsigned vo, const void* sb) { asm volatile("global_load_dword %0, %1, %2" : "=v"(d) : "v"(vo), "s"(sb) : "memory"); }
DEVI void keepu(unsigned& v) { asm volatile("" : "+v"(v)); }
DEVI void wait_lgkm0() { asm volatile("s_waitcnt lgkmcnt(0)" ::: "memory"); }
DEVI void cbar() { asm volatile("" ::: "memory"); }
template <int N> DEVI void wait_vm() { asm volatile("s_waitcnt vmcnt(%0)" ::"n"(N) : "memory"); }
DEVI void bar_lgkm() { asm volatile("s_waitcnt lgkmcnt(0)\n\ts_barrier" ::: "memory"); }
template <int N> DEVI void bar_vm() { asm volatile("s_waitcnt vmcnt(%0) lgkmcnt(0)\n\ts_barrier" ::"n"(N) : "memory"); }
template <class T> DEVI T ldsr(unsigned a) { return *(LDS3 T*)(uintptr_t)a; }
template <class T> DEVI void ldsw(unsigned a, T v) { *(LDS3 T*)(uintptr_t)a = v; }
DEVI int tau(int r) { return 16 * ((r >> 2) & 1) + (r & 3) + 4 * (r >> 3); }
template <int C> DEVI float dpp_add(float v) {
  return v + __uint_as_float(__builtin_amdgcn_update_dpp(0u, __float_as_uint(v), C, 0xf, 0xf, true));
}
DEVI void swap16(float v, float& even, float& odd) {
  auto s = __builtin_amdgcn_permlane16_swap(__float_as_uint(v), __float_as_uint(v), false, false);
  even = __uint_as_float(s[0]); odd = __uint_as_float(s[1]);
}
DEVI void swap32(float v, float& lo, float& hi) {
  auto s = __builtin_amdgcn_permlane32_swap(__float_as_uint(v), __float_as_uint(v), false, false);
  lo = __uint_as_float(s[0]); hi = __uint_as_float(s[1]);
}

#define TS(i)

constexpr int SLOT = 16384;
constexpr int L_D3 = 9 * SLOT;
constexpr int L_SEG = L_D3 + 8192;
constexpr int L_PAR = L_SEG + 1024;
constexpr int L_RS = L_PAR + 512;
constexpr int L_TOT = L_RS + 1024;
constexpr int SPIN = 1 << 18;

struct Args {
  const unsigned short *x, *lre, *lim, *bre, *bim, *cre, *cim, *ldt, *dsk, *wg, *wu, *wd;
  char* ws;
  float* out;
};

constexpr int kstep_of(int s) { return s < 16 ? s : (s < 20 ? s + 8 : (s < 28 ? s - 4 : (s - 28) % 28)); }
constexpr int wslot(int n) { return (n + 1) % 9; }
constexpr int PA = 6;
constexpr int young2(int s) {
  int n = 2 * ((s + PA - 1 < 27 ? s + PA - 1 : 27) - s);
  for (int t = (s - PA > 0 ? s - PA : 0); t < s; ++t) n += t < 12 ? 2 : 0;
  return n;
}

__global__ __launch_bounds__(512, 2) void fused_kernel(Args a) {
  __shared__ __attribute__((aligned(16))) char smem[L_TOT];
  const unsigned lb = (unsigned)(uintptr_t)(LDS3 char*)smem;
  const int xcd = blockIdx.x & 7, idx = blockIdx.x >> 3;
  const int w = __builtin_amdgcn_readfirstlane(threadIdx.x >> 6), lane = threadIdx.x & 63;
  unsigned cpf = 0;
  {
    const char* pc = (const char*)__builtin_amdgcn_s_getpc();
    gld4(cpf, min(w * 64 + lane, CODE_LINES - 1) * 128, pc);
  }
  if (idx >= 28) {
    unsigned acc = 0;
    const int p = (idx - 28) * 8 + w;
#pragma unroll 2
    for (int it = 0; it < 10; ++it) {
      int ln = min((it * 32 + p) * 64 + lane, 18815), qd = ln / 2688, q2 = ln - qd * 2688, kr = q2 / 448, q3 = q2 - kr * 448;
      const unsigned short* src = kr >= 2 ? a.wd + (size_t)(64 * (7 * (kr - 2) + qd)) * DM + q3 * 64
                                          : (kr ? a.cim : a.cre) + (size_t)q3 * DM + qd * 64;
      acc ^= *(const unsigned*)src;
    }
    useu(acc);
    useu(cpf);
    return;
  }
  const int nt = idx % 7, jg = xcd & 1, c = 4 * (xcd >> 1) + idx / 7, j = 7 * jg + nt;
  const int mq = w & 3, nh = w >> 2;
  const int r = lane & 31, h = lane >> 5, o16 = (lane >> 4) & 1, c16 = lane & 15, dist = lane >> 4;
  const char* const wsA = a.ws + OFF_A + (size_t)((4 * c + mq) * KS_ALL) * 1024;
  TS(0);
  unsigned p_re = 0, p_im = 0, p_dt = 0;
  u32x4 raw[2][7];
  if (w == 0) {
    asm volatile("global_load_ushort %0, %1, %2" : "=v"(p_re) : "v"((32 * j + r) * 2), "s"(a.lre) : "memory");
    asm volatile("global_load_ushort %0, %1, %2" : "=v"(p_im) : "v"((32 * j + r) * 2), "s"(a.lim) : "memory");
    asm volatile("global_load_ushort %0, %1, %2" : "=v"(p_dt) : "v"((32 * j + r) * 2), "s"(a.ldt) : "memory");
  }
  int rhog[2];
  const char* const xb = (const char*)a.x + (size_t)(128 * c) * 896;
#pragma unroll
  for (int g = 0; g < 2; ++g) {
    const int rho = 8 * ((8 * nh + 4 * g + mq + 2 * nt + 1) & 15) + (lane >> 3);
    rhog[g] = rho;
    const unsigned vo = ((rho & 96) + tau(rho & 31)) * 896 + (lane & 7) * 16;
    static_for<7>([&](auto I) { gld16<I * 128>(raw[g][I], vo, xb); });
  }
  const unsigned vq = (lane >> 3) * 896 + (((lane & 7) ^ ((4 * w + (lane >> 4)) & 7)) << 4);
  const char* const bb = (const char*)((w & 2) ? a.bim : a.bre) + (32 * j + 8 * (w & 1)) * 896;
  const char* const wb = (const char*)(nh ? a.wu : a.wg) + 256 * j;
  const char* const inbox = a.ws + OFF_AGG + (size_t)(c * 14 + j) * 8192;
  unsigned warm = 0, warm2 = 0;
  if (nh == 1) {
    static_for<5>([&](auto I) {
      constexpr int slab = I < 4 ? I : 6;
      glds1<7 * SLOT + I * 8192>(vq, bb + slab * 128, lb + mq * 1024);
      glds1<7 * SLOT + I * 8192 + 4096>(vq, bb + slab * 128 + 16 * 896, lb + mq * 1024);
    });
  }
  TS(12);
#pragma unroll
  for (int g = 0; g < 2; ++g) {
    if (nh == 0) { if (g == 0) wait_vm<7>(); else wait_vm<0>(); }
    if (g == 0) wait7<17>(raw[0]); else wait7<10>(raw[1]);
    if (g == 0) { TS(1); } else { TS(14); }
    float s1a = 0.f, s2a = 0.f, s1b = 0.f, s2b = 0.f;
#pragma unroll
    for (int i = 0; i < 7; ++i) {
      s1a = dot2(raw[g][i][0], 0x3f803f80u, s1a); s2a = dot2(raw[g][i][0], raw[g][i][0], s2a);
      s1b = dot2(raw[g][i][1], 0x3f803f80u, s1b); s2b = dot2(raw[g][i][1], raw[g][i][1], s2b);
      s1a = dot2(raw[g][i][2], 0x3f803f80u, s1a); s2a = dot2(raw[g][i][2], raw[g][i][2], s2a);
      s1b = dot2(raw[g][i][3], 0x3f803f80u, s1b); s2b = dot2(raw[g][i][3], raw[g][i][3], s2b);
    }
    float s1 = s1a + s1b, s2 = s2a + s2b;
    s1 = dpp_add<0xB1>(s1); s2 = dpp_add<0xB1>(s2);
    s1 = dpp_add<0x4E>(s1); s2 = dpp_add<0x4E>(s2);
    s1 = dpp_add<0x141>(s1); s2 = dpp_add<0x141>(s2);
    const float mean = s1 * (1.0f / DM);
    const float rstd = __builtin_amdgcn_rsqf(fmaxf(s2 * (1.0f / DM) - mean * mean, 0.f) + 1e-5f);
    const float nb = -mean * rstd;
    const int rho = rhog[g];
    const unsigned wa = lb + rho * 128 + (((lane & 7) ^ ((rho >> 1) & 7)) << 4);
    const f32x2 r2 = {rstd, rstd}, n2 = {nb, nb};
#pragma unroll
    for (int i = 0; i < 7; ++i) {
      f32x2 t[4];
#pragma unroll
      for (int e = 0; e < 4; ++e) t[e] = f32x2{__uint_as_float(raw[g][i][e] << 16), __uint_as_float(raw[g][i][e] & 0xffff0000u)};
      __builtin_amdgcn_sched_barrier(0);
#pragma unroll
      for (int e = 0; e < 4; ++e) t[e] = __builtin_elementwise_fma(t[e], r2, n2);
      __builtin_amdgcn_sched_barrier(0);
      u32x4 ov;
#pragma unroll
      for (int e = 0; e < 4; ++e) ov[e] = pack2(t[e][0], t[e][1]);
      ldsw(wa + i * SLOT, ov);
    }
    ldsw(lb + L_RS + rho * 8, f32x2{rstd, nb});
  }
  useu(cpf);
  TS(2);
  {
    int ln = min(mq * 64 + lane, 223) * 4 + (c & 3), un = ln >= 448, row = ln - 448 * un;
    asm volatile("global_load_dword %0, %1, %2" : "=v"(warm) : "v"(row * 3584 + un * 128), "s"(wb) : "memory");
  }
  if (w == 0) {
    asm volatile("" : "+v"(p_re), "+v"(p_im), "+v"(p_dt));
    float dt = expf(bf2f(p_dt)), are = bf2f(p_re), aim = bf2f(p_im);
    float e = expf(dt * are), sn, cn;
    sincosf(dt * aim, &sn, &cn);
    float lr = e * cn, li = e * sn;
    float inv = 1.0f / (are * are + aim * aim);
    float nr = lr - 1.0f, ni = li;
    f32x4 pv = {lr, li, (nr * are + ni * aim) * inv, (ni * are - nr * aim) * inv};
    ldsw(lb + L_PAR + r * 16, pv);
  }
  TS(13);
  const unsigned lw1 = lb + w * 1024, lw2 = lb + w * 2048;
  const char* const bb2 = bb + nh * (16 * 896);
  const unsigned vw0 = (16 * mq + (lane >> 3)) * 3584 + (((lane & 7) ^ (2 * (2 * nh + ((lane >> 4) & 1)))) << 4);
  const unsigned vw1 = vw0 + 8 * 3584 - 1024;
  auto stage_w_ = [&](auto N) {
    constexpr int n = N;
    glds2<wslot(n) * SLOT>(vw0, vw1, wb + (n / 7) * 128 + (n % 7) * (64 * 3584), lw2);
  };
  const char* pd = (const char*)a.wd + nt * 128 + w * (8 * 896);
  const char* pc0 = (const char*)a.cre + (64 * nt + 8 * w) * 896;
  const char* pc1 = (const char*)a.cim + (64 * nt + 8 * w) * 896;
  asm volatile("" : "+s"(pd), "+s"(pc0), "+s"(pc1));
  auto stage_u = [&](auto U) {
    constexpr int u = U, kr = u / 3, qd = u % 3;
    glds1<u * 8192>(vq, kr >= 2 ? pd + (64 * (7 * (kr - 2) + qd)) * 896 : (kr ? pc1 : pc0) + qd * 128, lw1);
  };
#define stage_w(n) stage_w_(IC(n))
#define stage_p(sl) { stage_u(IC(2 * (sl))); stage_u(IC(2 * (sl) + 1)); }

  unsigned toff[4];
#pragma unroll
  for (int ks = 0; ks < 4; ++ks) toff[ks] = lb + r * 128 + (((2 * ks + h) ^ ((r >> 1) & 7)) << 4);
  const int q4 = (lane >> 2) & 3;
  unsigned wta[2][3];
#pragma unroll
  for (int t = 0; t < 2; ++t) {
    wta[t][0] = lb + o16 * 8192 + (8 * h + q4) * 128 + (((2 * nh + t) ^ (2 * o16 + (q4 >> 1))) << 5) + (lane & 3) * 8;
    wta[t][1] = wta[t][0] + 3 * SLOT; wta[t][2] = wta[t][0] + 6 * SLOT;
    keepu(wta[t][1]); keepu(wta[t][2]);
  }

  {
  bf16x8 xf[28];
  bf16x8 fs[4], fr[3][2];
  f32x16 accs = {}, accu[2][2] = {};
  float lr, li, cfr, cfi, l64r, l64i, lcr, lci, l2r, l2i, l4r, l4i, yr = 0.f, yi = 0.f, t0r, t0i, t1r, t1i;
  float l8r, l8i, l16r, l16i, l32r, l32i, clr = 0.f, cli = 0.f, er = 0.f, ei = 0.f;
  float ur[8], ui[8];
  u32x4 gr[4], ov[2];
  float pbr, pbi;
  const unsigned segb = lb + L_SEG + nh * 512 + c16 * 8;
  f32x2 sg[4];
  const char* const pubb = a.ws + OFF_AGG + (4 * mq * 14 + j) * 8192 + c * 512 + nh * 256;
  auto publish = [&]() {
    int l2 = lane; keepi(l2);
    st16((l2 >> 4) * (14 * 8192) + (l2 & 15) * 16, pubb, u32x4{__float_as_uint(pbr), 1u, __float_as_uint(pbi), 1u});
  };

  auto load_x = [&](auto KI) {
    constexpr int k = KI;
    xf[k] = lds_b128(toff[k & 3] + mq * 4096 + (k >> 2) * SLOT);
  };
  auto load_s = [&](auto SI) {
    constexpr int s = SI, k = kstep_of(s), sl = k >> 2;
    constexpr int off = sl < 4 ? 7 * SLOT + sl * 8192 : (sl == 6 ? L_D3 : (sl - 4) * 8192);
    fs[s & 3] = lds_b128(toff[k & 3] + nh * 4096 + off);
  };
  auto load_w = [&](auto SI, int t) {
    constexpr int s = SI;
    if constexpr (s < 84) {
      constexpr int n = (s - 28) >> 2, ks = s & 3, sl = wslot(n);
      unsigned ad = wta[t][sl / 3] + (sl % 3) * SLOT + ks * 2048;
      fr[s % 3][t] = lds_tr(ad, ad + 512);
    }
  };
  auto clear_inbox = [&]() {
    u32x4 z = {0u, 0u, 0u, 0u};
    st16(w == 6 && lane < 8 ? (unsigned)(OFF_FLG - OFF_AGG) + lane * 16 - (c * 14 + j) * 8064 : w * 1024 + lane * 16, inbox, z);
    st16(w * 1024 + lane * 16, inbox, z);
  };
  auto poll = [&]() {
    int l2 = lane; keepi(l2);
#pragma unroll
    for (int n = 0; n < 4; ++n) gld16_sc1(gr[n], ((c - 1 - 4 * n - (l2 >> 4)) & 15) * 512 + (l2 & 15) * 16, inbox + nh * 256);
  };
  auto act1 = [&](float g, float u) { return (g * u) * __builtin_amdgcn_rcpf(1.0f + __builtin_amdgcn_exp2f(g * -1.44269504f)); };
  auto epi2 = [&](const f32x16& ac, int i) { return pack2(act1(ac[2 * i], ac[8 + 2 * i]), act1(ac[2 * i + 1], ac[9 + 2 * i])); };

#define AT(x, q0, d, stmt) if constexpr (x == (q0)) { stmt; }
  auto hook = [&](auto QI) {
    constexpr int q = QI;
    if constexpr (q >= 2 && q < 18) {
      constexpr int i = (q - 2) >> 1;
      if constexpr ((q & 1) == 0) {
        auto sw = __builtin_amdgcn_permlane16_swap(__float_as_uint(accs[i]), __float_as_uint(accs[i + 8]), false, false);
        float vr = __uint_as_float(sw[0]), vi = __uint_as_float(sw[1]);
        cmul(ur[i], ui[i], cfr, cfi, vr, vi);
      } else {
        cfma(er, ei, lr, li, er, ei, ur[i], ui[i]);
      }
    }
    AT(q, 0, 2, stage_w(5)) AT(q, 5, 3, stage_w(6))
    AT(q, 12, 2, stage_w(7)) AT(q, 17, 2, stage_w(8)) AT(q, 22, 3, stage_w(9))
    AT(q, 28, 2, stage_w(10)) AT(q, 33, 2, publish()) AT(q, 37, 2, stage_w(11)) AT(q, 41, 2, stage_w(12))
    AT(q, 45, 3, stage_w(13))
    AT(q, 60, 2, stage_p(7)) AT(q, 64, 6, stage_p(8))
    AT(q, 66, 2, st16(lane * 16, wsA + (56 + 8 * j + 2 * nh) * 1024, ov[0]))
    if constexpr (q == 72) poll();
    AT(q, 74, 1, st16(lane * 16, wsA + (56 + 8 * j + 2 * nh + 1) * 1024, ov[1]))
    AT(q, 76, 2, stage_p(0)) AT(q, 83, 2, keepv(pbr); publish()) AT(q, 87, 3, stage_p(1))
    AT(q, 92, 3, stage_p(2)) AT(q, 98, 3, stage_p(3))
    if constexpr (q == 18) { swap16(er, t0r, yr); swap16(ei, t0i, yi); pin2(t0r, yr); pin2(t0i, yi); }
    if constexpr (q == 19) cfma(er, ei, l8r, l8i, t0r, t0i, yr, yi);
    if constexpr (q == 20) { swap32(er, t1r, yr); swap32(ei, t1i, yi); pin2(t1r, yr); pin2(t1i, yi); }
    if constexpr (q == 21) {
      cfma(yr, yi, l16r, l16i, t1r, t1i, yr, yi);
      ldsw(segb + mq * 128, f32x2{yr, yi});
    }
    if constexpr (q == 23) { t1r = h ? t1r : 0.f; t1i = h ? t1i : 0.f; cfma(yr, yi, l8r, l8i, t1r, t1i, t0r, t0i); }
    if constexpr (q == 24) { clr = o16 ? yr : t1r; cli = o16 ? yi : t1i; pin2(clr, cli); csq(l32r, l32i, l64r, l64i); }
    if constexpr (q == 25) { yr = h ? l16r : 1.f; yi = h ? l16i : 0.f; t1r = o16 ? l8r : 1.f; t1i = o16 ? l8i : 0.f; pin2(yr, yi); pin2(t1r, t1i); }
    if constexpr (q == 26) cmul(yr, yi, yr, yi, t1r, t1i);
    if constexpr (q == 28) {
#pragma unroll
      for (int i = 0; i < 4; ++i) sg[i] = ldsr<f32x2>(segb + i * 128);
    }
    if constexpr (q == 30) cfma(t0r, t0i, l32r, l32i, sg[0][0], sg[0][1], sg[1][0], sg[1][1]);
    if constexpr (q == 31) cfma(t1r, t1i, l32r, l32i, t0r, t0i, sg[2][0], sg[2][1]);
    if constexpr (q == 32) cfma(pbr, pbi, l32r, l32i, t1r, t1i, sg[3][0], sg[3][1]);
    if constexpr (q == 34) {
      er = mq == 0 ? 0.f : (mq == 1 ? sg[0][0] : (mq == 2 ? t0r : t1r));
      ei = mq == 0 ? 0.f : (mq == 1 ? sg[0][1] : (mq == 2 ? t0i : t1i));
      pin2(er, ei);
    }
    if constexpr (q == 35) cfma(clr, cli, yr, yi, er, ei, clr, cli);
    if constexpr (q == 36) { t1r = (mq & 1) ? l32r : 1.f; t1i = (mq & 1) ? l32i : 0.f; cmul(yr, yi, yr, yi, t1r, t1i); }
    if constexpr (q == 37) { t0r = (mq & 2) ? l64r : 1.f; t0i = (mq & 2) ? l64i : 0.f; cmul(t0r, t0i, t0r, t0i, yr, yi); }
    if constexpr (q >= 58 && q < 74) {
      constexpr int e = q - 58, t = e >> 3, i = e & 7;
      if constexpr ((i & 1) == 0) { t1r = act1(accu[0][t][i], accu[0][t][8 + i]); keepv(t1r); }
      else { unsigned pk = pack2(t1r, act1(accu[0][t][i], accu[0][t][8 + i])); keepu(pk); ov[t][i >> 1] = pk; }
    }
    if constexpr (q == 77) csq(l64r, l64i, lcr, lci);
    if constexpr (q == 79) csq(lcr, lci, l2r, l2i);
    if constexpr (q == 80) csq(l2r, l2i, l4r, l4i);
    if constexpr (q == 81) {
      wait4<3>(gr);
      for (int spin = 0; spin < SPIN; ++spin) {
        unsigned ok = (gr[0][1] & gr[0][3] & gr[1][1]) & (gr[1][3] & gr[2][1] & gr[2][3]) & (gr[3][1] & gr[3][3]);
        if (__all(ok != 0u)) break;
        __builtin_amdgcn_s_sleep(2);
        poll();
        wait4<0>(gr);
      }
      TS(29);
    }
    if constexpr (q >= 82 && q < 86) {
      constexpr int n = 85 - q;
      bool v = 4 * n + dist < c;
      float xr = v ? __uint_as_float(gr[n][0]) : 0.f, xi = v ? __uint_as_float(gr[n][2]) : 0.f;
      if constexpr (n == 3) { yr = xr; yi = xi; pin2(yr, yi); }
      else cfma(yr, yi, l4r, l4i, yr, yi, xr, xi);
    }
    if constexpr (q == 87) { swap16(yr, er, t1r); swap16(yi, ei, t1i); pin2(er, t1r); pin2(ei, t1i); }
    if constexpr (q == 88) cfma(yr, yi, lcr, lci, t1r, t1i, er, ei);
    if constexpr (q == 89) { swap32(yr, er, t1r); swap32(yi, ei, t1i); pin2(er, t1r); pin2(ei, t1i); }
    if constexpr (q == 90) cfma(yr, yi, l2r, l2i, t1r, t1i, er, ei);
    if constexpr (q == 91) cfma(er, ei, t0r, t0i, yr, yi, clr, cli);
    if constexpr (q >= 93 && q < 101) {
      constexpr int i = q - 93;
      cfma(er, ei, lr, li, er, ei, ur[i], ui[i]);
      ldsw(lb + 6 * SLOT + w * 2048 + ((i & 3) + 8 * (i >> 2) + 16 * o16 + 4 * h) * 64 + ((c16 * 4) ^ (((2 * (i >> 2) + h) & 3) << 4)), pack2(er, -ei));
    }
    if constexpr (q == 101) {
      cbar();
#pragma unroll
      for (int e = 0; e < 2; ++e) ov[e] = ldsr<u32x4>(lb + 6 * SLOT + w * 2048 + r * 64 + (((2 * h + e) ^ ((r >> 2) & 3)) << 4));
    }
    if constexpr (q == 103) {
#pragma unroll
      for (int i = 0; i < 4; ++i) {
        gr[0][i] = __builtin_amdgcn_perm(ov[i >> 1][2 * (i & 1) + 1], ov[i >> 1][2 * (i & 1)], 0x05040100u);
        gr[1][i] = __builtin_amdgcn_perm(ov[i >> 1][2 * (i & 1) + 1], ov[i >> 1][2 * (i & 1)], 0x07060302u);
      }
    }
    AT(q, 104, 2, st16(lane * 16, wsA + (2 * j + nh) * 1024, gr[0]); st16(lane * 16, wsA + (28 + 2 * j + nh) * 1024, gr[1]))
    if constexpr (q >= 108 && q < 111) stage_p(q - 104);
  };

  bar_vm<1>();
  TS(3);
  static_for<4>(load_x);
  static_for<3>(load_s);

  static_for<84>([&](auto SI) {
    constexpr int s = SI;
    if constexpr (s == 3 || s == 6) { bar_lgkm(); TS(s == 3 ? 4 : 5); }
    if constexpr (s == 17) { bar_vm<6>(); useu(warm); TS(6); }
    if constexpr (s == 26) { bar_vm<6>(); TS(7); }
    if constexpr (s == 34) { bar_vm<6>(); TS(8); }
    if constexpr (s == 42) { bar_vm<8>(); TS(9); }
    if constexpr (s == 50) { bar_vm<11>(); TS(10); }
    if constexpr (s == 58) { bar_vm<0>(); TS(11); }
    if constexpr (s == 66 || s == 74 || s == 82) { bar_lgkm(); TS(26 + (s - 66) / 8); }
    if constexpr (s < 28) {
      if constexpr (s < 6) static_for<4>([&](auto I) { load_x(std::integral_constant<int, 4 * (s + 1) + I>{}); });
      if constexpr (s + 3 < 28) load_s(std::integral_constant<int, s + 3>{});
      if constexpr (s >= 26) { load_w(std::integral_constant<int, s + 2>{}, 0); load_w(std::integral_constant<int, s + 2>{}, 1); }
      __builtin_amdgcn_sched_barrier(0);
      accs = mfma(xf[kstep_of(s)], fs[s & 3], accs);
      __builtin_amdgcn_sched_barrier(0);
      AT(s, 3, 1, glds1<0>(vq, bb2 + 512, lw1); glds1<8192>(vq, bb2 + 640, lw1))
      AT(s, 6, 2, stage_w(0)) AT(s, 10, 2, stage_w(1))
      AT(s, 13, 2, clear_inbox())
      AT(s, 17, 2, stage_w(2)) AT(s, 21, 3, stage_w(3)) AT(s, 26, 1, stage_w(4))
      if constexpr (s == 16) {
        f32x4 pv = ldsr<f32x4>(lb + L_PAR + (16 * nh + c16) * 16);
        lr = pv[0]; li = pv[1]; cfr = pv[2]; cfi = pv[3];
      }
      if constexpr (s == 18) csq(lr, li, t0r, t0i);
      if constexpr (s == 20) csq(t0r, t0i, t1r, t1i);
      if constexpr (s == 22) csq(t1r, t1i, l8r, l8i);
      if constexpr (s == 23) csq(l8r, l8i, l16r, l16i);
      if constexpr (s == 25) csq(l16r, l16i, l32r, l32i);
      __builtin_amdgcn_sched_barrier(0);
    } else {
      constexpr int u = (s - 28) / 28;
      accu[u][0] = mfma(fr[s % 3][0], xf[kstep_of(s)], accu[u][0]);
      __builtin_amdgcn_sched_barrier(0);
      load_w(std::integral_constant<int, s + 2>{}, 0);
      hook(std::integral_constant<int, 2 * (s - 28)>{});
      __builtin_amdgcn_sched_barrier(0);
      accu[u][1] = mfma(fr[s % 3][1], xf[kstep_of(s)], accu[u][1]);
      __builtin_amdgcn_sched_barrier(0);
      load_w(std::integral_constant<int, s + 2>{}, 1);
      hook(std::integral_constant<int, 2 * (s - 28) + 1>{});
      __builtin_amdgcn_sched_barrier(0);
    }
  });
  TS(15);
#pragma unroll
  for (int t = 0; t < 2; ++t) {
#pragma unroll
    for (int i = 0; i < 4; ++i) ov[t][i] = epi2(accu[1][t], i);
    st16(lane * 16, wsA + (56 + 8 * j + 4 + 2 * nh + t) * 1024, ov[t]);
  }
  }
  TS(16);
  bar_vm<0>();
  TS(17);

  int tid = threadIdx.x;
  asm volatile("" : "+v"(tid));
  const int c4 = tid & 15;
  u32x2 xv[2];
  const u32x2 dk = *(const u32x2*)(a.dsk + 64 * nt + 4 * c4);
  int trow[2];
#pragma unroll
  for (int e = 0; e < 2; ++e) {
    trow[e] = 128 * c + 64 * jg + 32 * e + tau(tid >> 4);
    xv[e] = *(const u32x2*)(a.x + (size_t)trow[e] * DM + 64 * nt + 4 * c4);
  }
  if (w < 6) {
    const int lane = tid & 63, r = lane & 31, h = lane >> 5, o16 = (lane >> 4) & 1, q4 = (lane >> 2) & 3;
    unsigned toff[4];
#pragma unroll
    for (int ks = 0; ks < 4; ++ks) toff[ks] = lb + r * 128 + (((2 * ks + h) ^ ((r >> 1) & 7)) << 4);
    bf16x8 bq[3][4][2];
    u32x4 af[PA][2];
    f32x16 acc[2][2] = {};
    const unsigned rg = w * 24576;
    unsigned tra[2][2];
#pragma unroll
    for (int n = 0; n < 2; ++n)
#pragma unroll
      for (int e = 0; e < 2; ++e)
        tra[n][e] = lb + rg + (4 * h + q4 + 8 * e) * 128 + (((4 * n + 2 * o16 + ((lane >> 1) & 1)) ^ ((2 * h + (q4 >> 1) + 4 * e) & 7)) << 4) + (lane & 1) * 8;
    auto read_quad = [&](int u) {
      if (w < 2) {
#pragma unroll
        for (int i = 0; i < 4; ++i)
#pragma unroll
          for (int n = 0; n < 2; ++n) bq[u][i][n] = lds_b128(toff[i] + rg + u * 8192 + n * 4096);
      } else {
#pragma unroll
        for (int i = 0; i < 4; ++i)
#pragma unroll
          for (int n = 0; n < 2; ++n) bq[u][i][n] = lds_tr(tra[n][0] + u * 8192 + i * 2048, tra[n][1] + u * 8192 + i * 2048);
      }
    };
    const unsigned vq0 = (lane >> 3) * 896 + (((lane & 7) ^ (lane >> 4)) << 4);
    const unsigned vq1 = ((lane >> 3) + 8) * 896 + (((lane & 7) ^ (4 + (lane >> 4))) << 4) - 1024;
    const char* const qsrc = w >= 2 ? (const char*)a.wd + (size_t)(448 * (w - 2)) * 896 + nt * 128
                                    : (const char*)(w ? a.cim : a.cre) + (64 * nt) * 896;
    const int qstr = w >= 2 ? 64 * 896 : 128;
    auto stage_quad = [&](auto QD) {
      constexpr int qd = QD;
      static_for<4>([&](auto I) { glds2<(qd % 3) * 8192 + I * 2048>(vq0, vq1, qsrc + qd * qstr + I * (16 * 896), lb + rg); });
    };
    read_quad(0); read_quad(1); read_quad(2);
    asm volatile("s_waitcnt lgkmcnt(0)" ::: "memory");
    __builtin_amdgcn_sched_barrier(0);
    stage_quad(IC(3));
    TS(20);
    __builtin_amdgcn_s_barrier();
    TS(21);
    const char* const pa = a.ws + OFF_A + (size_t)((4 * c + 2 * jg) * KS_ALL + 28 * w) * 1024;
    const unsigned l0 = lane * 16, l1 = lane * 16 + KS_ALL * 1024;
    auto lda = [&](int s) {
      if (ACQ) { gld16<0>(af[s % PA][0], l0, pa + s * 1024); gld16<0>(af[s % PA][1], l1, pa + s * 1024); }
      else { gld16_sc1(af[s % PA][0], l0, pa + s * 1024); gld16_sc1(af[s % PA][1], l1, pa + s * 1024); }
    };
#pragma unroll
    for (int s = 0; s < PA; ++s) lda(s);
    static_for<28>([&](auto SI) {
      constexpr int s = SI;
      wait2<young2(s)>(af[s % PA][0], af[s % PA][1]);
#pragma unroll
      for (int m = 0; m < 2; ++m)
#pragma unroll
        for (int n = 0; n < 2; ++n)
          acc[m][n] = mfma(bq[(s >> 2) % 3][s & 3][n], __builtin_bit_cast(bf16x8, af[s % PA][m]), acc[m][n]);
      __builtin_amdgcn_sched_barrier(0);
      if constexpr (s + PA < 28) lda(s + PA);
      if constexpr (s == 8) wait_lgkm0();
      if constexpr (s < 12) {
        constexpr int qd = 4 + (s >> 2), i = s & 3;
        glds2<(qd % 3) * 8192 + i * 2048>(vq0, vq1, qsrc + qd * qstr + i * (16 * 896), lb + rg);
      }
      if constexpr (s == 3) read_quad(0);
      if constexpr (s == 11 || s == 15 || s == 19) read_quad(((s - 7) >> 2) % 3);
      if constexpr (s == 0) { TS(22); }
      __builtin_amdgcn_sched_barrier(0);
    });
    TS(23);
    const unsigned pw = lb + rg + r * 272 + h * 16;
#pragma unroll
    for (int m = 0; m < 2; ++m)
#pragma unroll
      for (int n = 0; n < 2; ++n)
#pragma unroll
        for (int g = 0; g < 4; ++g) {
          f32x4 v = {acc[m][n][4 * g], acc[m][n][4 * g + 1], acc[m][n][4 * g + 2], acc[m][n][4 * g + 3]};
          ldsw(pw + m * 32 * 272 + n * 128 + g * 32, v);
        }
  } else {
    if (w == 6) {
      const int ln = min(lane, 13);
      GLB1 unsigned* const fo = (GLB1 unsigned*)(a.ws + OFF_FLG + (c * 14 + ln) * 128 + j * 4);
      GLB1 unsigned* const fi = (GLB1 unsigned*)(a.ws + OFF_FLG + (c * 14 + j) * 128 + ln * 4);
      __hip_atomic_store(fo, 1u, __ATOMIC_RELAXED, __HIP_MEMORY_SCOPE_AGENT);
      for (int spin = 0; spin < SPIN; ++spin) {
        unsigned v = __hip_atomic_load(fi, __ATOMIC_RELAXED, __HIP_MEMORY_SCOPE_AGENT);
        if (__all(v == 1u)) break;
        __builtin_amdgcn_s_sleep(1);
      }
      TS(20);
      if (ACQ) {
        __builtin_amdgcn_fence(__ATOMIC_ACQUIRE, "agent");
        asm volatile("s_waitcnt vmcnt(0)" ::: "memory");
      }
      asm volatile("s_barrier" ::: "memory");
      __hip_atomic_store(fo, 1u, __ATOMIC_RELAXED, __HIP_MEMORY_SCOPE_AGENT);
    } else {
      TS(20);
      asm volatile("s_barrier" ::: "memory");
    }
    TS(21); TS(22); TS(23);
  }
  bar_lgkm();
  TS(24);
#pragma unroll
  for (int e = 0; e < 2; ++e) {
    const int row = (tid >> 4) + 32 * e;
    const unsigned pr = lb + row * 272 + c4 * 16;
    f32x4 sv = ldsr<f32x4>(pr) + ldsr<f32x4>(pr + 24576);
    f32x4 dv = (ldsr<f32x4>(pr + 2 * 24576) + ldsr<f32x4>(pr + 3 * 24576)) + (ldsr<f32x4>(pr + 4 * 24576) + ldsr<f32x4>(pr + 5 * 24576));
    const f32x2 st = ldsr<f32x2>(lb + L_RS + (64 * jg + row) * 8);
    f32x4 o;
#pragma unroll
    for (int i = 0; i < 4; ++i) {
      unsigned xw = xv[e][i >> 1], dw = dk[i >> 1];
      float xf = __uint_as_float((i & 1) ? (xw & 0xffff0000u) : (xw << 16));
      float df = __uint_as_float((i & 1) ? (dw & 0xffff0000u) : (dw << 16));
      o[i] = xf + (2.0f * sv[i] + fmaf(xf, st[0], st[1]) * df) * silu(dv[i]);
    }
    st16((trow[e] * DM + 64 * nt + 4 * c4) * 4, a.out, __builtin_bit_cast(u32x4, o));
  }
}

extern "C" void kernel_launch(void* const* d_in, const int* in_sizes, int n_in, void* d_out, int out_size,
                              void* d_ws, size_t ws_size, hipStream_t stream) {
  Args a;
  a.x = (const unsigned short*)d_in[0];
  a.lre = (const unsigned short*)d_in[1];
  a.lim = (const unsigned short*)d_in[2];
  a.bre = (const unsigned short*)d_in[3];
  a.bim = (const unsigned short*)d_in[4];
  a.cre = (const unsigned short*)d_in[5];
  a.cim = (const unsigned short*)d_in[6];
  a.ldt = (const unsigned short*)d_in[7];
  a.dsk = (const unsigned short*)d_in[8];
  a.wg = (const unsigned short*)d_in[10];
  a.wu = (const unsigned short*)d_in[11];
  a.wd = (const unsigned short*)d_in[12];
  a.ws = (char*)d_ws;
  a.out = (float*)d_out;
  fused_kernel<<<256, 512, 0, stream>>>(a);
}
```

```cpp
#include <hip/hip_runtime.h>
#include <hip/hip_bf16.h>
#include <stdint.h>
#include <utility>

typedef __attribute__((ext_vector_type(8))) __bf16 bf16x8;
typedef __attribute__((ext_vector_type(4))) __bf16 bf16x4;
typedef __attribute__((ext_vector_type(2))) __bf16 bf16x2;
typedef __attribute__((ext_vector_type(16))) float f32x16;
typedef __attribute__((ext_vector_type(4))) float f32x4;
typedef __attribute__((ext_vector_type(2))) float f32x2;
typedef __attribute__((ext_vector_type(4))) unsigned u32x4;
typedef __attribute__((ext_vector_type(2))) unsigned u32x2;
typedef unsigned long long u64;
#define LDS3 __attribute__((address_space(3)))
#define GLB1 __attribute__((address_space(1)))
#define DEVI __device__ __forceinline__

constexpr int DM = 448, FF = 1792;
constexpr int KS_ALL = 168;
constexpr size_t OFF_AGG = 0;
constexpr size_t OFF_FLG = 224 * 8192;
constexpr size_t OFF_A = 2097152;

template <int... Is, class Fn>
DEVI void static_for_impl(std::integer_sequence<int, Is...>, Fn&& f) {
  (f(std::integral_constant<int, Is>{}), ...);
}
template <int N, class Fn>
DEVI void static_for(Fn&& f) { static_for_impl(std::make_integer_sequence<int, N>{}, f); }

template <int OFF> DEVI void glds2(unsigned v0, unsigned v1, const void* sbase, unsigned base) {
  asm volatile("s_add_i32 m0, %3, %4\n\ts_nop 0\n\tglobal_load_lds_dwordx4 %0, %2\n\tglobal_load_lds_dwordx4 %1, %2 offset:1024"
               ::"v"(v0), "v"(v1), "s"(sbase), "s"(base), "n"(OFF) : "memory", "scc");
}
template <int OFF> DEVI void glds1(unsigned v0, const void* sbase, unsigned base) {
  asm volatile("s_add_i32 m0, %2, %3\n\ts_nop 0\n\tglobal_load_lds_dwordx4 %0, %1"
               ::"v"(v0), "s"(sbase), "s"(base), "n"(OFF) : "memory", "scc");
}
#define IC(n) std::integral_constant<int, (n)>{}
DEVI void cfma(float& o_r, float& o_i, float ar, float ai, float br, float bi, float cr, float ci) {
  float x, y;
  asm volatile("v_fma_f32 %0, %2, %4, %6\n\tv_fma_f32 %1, %2, %5, %7\n\tv_fma_f32 %0, -%3, %5, %0\n\tv_fma_f32 %1, %3, %4, %1"
               : "=&v"(x), "=&v"(y) : "v"(ar), "v"(ai), "v"(br), "v"(bi), "v"(cr), "v"(ci));
  o_r = x; o_i = y;
}
DEVI void cmul(float& o_r, float& o_i, float ar, float ai, float br, float bi) {
  float x, y;
  asm volatile("v_mul_f32 %0, %2, %4\n\tv_mul_f32 %1, %2, %5\n\tv_fma_f32 %0, -%3, %5, %0\n\tv_fma_f32 %1, %3, %4, %1"
               : "=&v"(x), "=&v"(y) : "v"(ar), "v"(ai), "v"(br), "v"(bi));
  o_r = x; o_i = y;
}
DEVI void csq(float ar, float ai, float& br, float& bi) { cmul(br, bi, ar, ai, ar, ai); }
DEVI void pin2(float& x, float& y) { asm volatile("" : "+v"(x), "+v"(y)); }
template <int OFF> DEVI void st16o(unsigned vo, const void* sb, u32x4 v) {
  asm volatile("global_store_dwordx4 %0, %1, %2 offset:%3 sc1\n\ts_nop 1" ::"v"(vo), "v"(v), "s"(sb), "n"(OFF) : "memory");
}
DEVI void st16(unsigned vo, const void* sb, u32x4 v) {
  asm volatile("global_store_dwordx4 %0, %1, %2 sc1\n\ts_nop 1" ::"v"(vo), "v"(v), "s"(sb) : "memory");
}
template <int N> DEVI void wait_vm_lgkm0() { asm volatile("s_waitcnt vmcnt(%0) lgkmcnt(0)" ::"n"(N) : "memory"); }
DEVI float bf2f(unsigned short v) { return __uint_as_float((unsigned)v << 16); }
DEVI unsigned pack2(float a, float b) {
  f32x2 p = {a, b};
  return __builtin_bit_cast(unsigned, __builtin_convertvector(p, bf16x2));
}
DEVI float dot2(unsigned a, unsigned b, float c) {
  return __builtin_amdgcn_fdot2_f32_bf16(__builtin_bit_cast(bf16x2, a), __builtin_bit_cast(bf16x2, b), c, false);
}
DEVI bf16x8 lds_b128(unsigned addr) { return *(LDS3 bf16x8*)(uintptr_t)addr; }
DEVI bf16x8 lds_tr(unsigned a0, unsigned a1) {
  bf16x4 a = __builtin_amdgcn_ds_read_tr16_b64_v4bf16((LDS3 bf16x4*)(uintptr_t)a0);
  bf16x4 b = __builtin_amdgcn_ds_read_tr16_b64_v4bf16((LDS3 bf16x4*)(uintptr_t)a1);
  return __builtin_shufflevector(a, b, 0, 1, 2, 3, 4, 5, 6, 7);
}
DEVI f32x16 mfma(bf16x8 a, bf16x8 b, f32x16 c) { return __builtin_amdgcn_mfma_f32_32x32x16_bf16(a, b, c, 0, 0, 0); }
DEVI float silu(float g) { return g * __builtin_amdgcn_rcpf(1.0f + __expf(-g)); }

template <int OFF> DEVI void gld16(u32x4& d, unsigned vo, const void* sb) {
  asm volatile("global_load_dwordx4 %0, %1, %2 offset:%3" : "=v"(d) : "v"(vo), "s"(sb), "n"(OFF) : "memory");
}
#define CODEPF 1
#define CODE_LINES 200
#define STAG 0
#define PRIO 0
#define ACQ 0
#define WARM2 1
DEVI void gld16_sc1(u32x4& d, unsigned vo, const void* sb) {
  asm volatile("global_load_dwordx4 %0, %1, %2 sc1" : "=v"(d) : "v"(vo), "s"(sb) : "memory");
}
template <int N> DEVI void wait4(u32x4 (&g)[4]) {
  asm volatile("s_waitcnt vmcnt(%4)" : "+v"(g[0]), "+v"(g[1]), "+v"(g[2]), "+v"(g[3]) : "n"(N) : "memory");
}
template <int N> DEVI void wait2(u32x4& x, u32x4& y) {
  asm volatile("s_waitcnt vmcnt(%2)" : "+v"(x), "+v"(y) : "n"(N) : "memory");
}
template <int N> DEVI void wait7(u32x4 (&g)[7]) {
  asm volatile("s_waitcnt vmcnt(%7)" : "+v"(g[0]), "+v"(g[1]), "+v"(g[2]), "+v"(g[3]), "+v"(g[4]), "+v"(g[5]), "+v"(g[6]) : "n"(N) : "memory");
}
DEVI void keepv(float& v) { asm volatile("" : "+v"(v)); }
DEVI void keepi(int& v) { asm volatile("" : "+v"(v)); }
DEVI void useu(unsigned v) { asm volatile("" ::"v"(v)); }
DEVI void gld4(unsigned& d, unsigned vo, const void* sb) { asm volatile("global_load_dword %0, %1, %2" : "=v"(d) : "v"(vo), "s"(sb) : "memory"); }
DEVI void keepu(unsigned& v) { asm volatile("" : "+v"(v)); }
DEVI void wait_lgkm0() { asm volatile("s_waitcnt lgkmcnt(0)" ::: "memory"); }
DEVI void cbar() { asm volatile("" ::: "memory"); }
template <int N> DEVI void wait_vm() { asm volatile("s_waitcnt vmcnt(%0)" ::"n"(N) : "memory"); }
DEVI void bar_lgkm() { asm volatile("s_waitcnt lgkmcnt(0)\n\ts_barrier" ::: "memory"); }
template <int N> DEVI void bar_vm() { asm volatile("s_waitcnt vmcnt(%0) lgkmcnt(0)\n\ts_barrier" ::"n"(N) : "memory"); }
template <class T> DEVI T ldsr(unsigned a) { return *(LDS3 T*)(uintptr_t)a; }
template <class T> DEVI void ldsw(unsigned a, T v) { *(LDS3 T*)(uintptr_t)a = v; }
DEVI int tau(int r) { return 16 * ((r >> 2) & 1) + (r & 3) + 4 * (r >> 3); }
template <int C> DEVI float dpp_add(float v) {
  return v + __uint_as_float(__builtin_amdgcn_update_dpp(0u, __float_as_uint(v), C, 0xf, 0xf, true));
}
DEVI void swap16(float v, float& even, float& odd) {
  auto s = __builtin_amdgcn_permlane16_swap(__float_as_uint(v), __float_as_uint(v), false, false);
  even = __uint_as_float(s[0]); odd = __uint_as_float(s[1]);
}
DEVI void swap32(float v, float& lo, float& hi) {
  auto s = __builtin_amdgcn_permlane32_swap(__float_as_uint(v), __float_as_uint(v), false, false);
  lo = __uint_as_float(s[0]); hi = __uint_as_float(s[1]);
}

#define TS(i)

constexpr int SLOT = 16384;
constexpr int L_D3 = 9 * SLOT;
constexpr int L_SEG = L_D3 + 8192;
constexpr int L_PAR = L_SEG + 1024;
constexpr int L_RS = L_PAR + 512;
constexpr int L_TOT = L_RS + 1024;
constexpr int SPIN = 1 << 18;

struct Args {
  const unsigned short *x, *lre, *lim, *bre, *bim, *cre, *cim, *ldt, *dsk, *wg, *wu, *wd;
  char* ws;
  float* out;
};

constexpr int kstep_of(int s) { return s < 16 ? s : (s < 20 ? s + 8 : (s < 28 ? s - 4 : (s - 28) % 28)); }
constexpr int wslot(int n) { return (n + 1) % 9; }
constexpr int PA = 6;
constexpr int young2(int s) {
  int n = 2 * ((s + PA - 1 < 27 ? s + PA - 1 : 27) - s);
  for (int t = (s - PA > 0 ? s - PA : 0); t < s; ++t) n += t < 12 ? 2 : 0;
  return n;
}

__global__ __launch_bounds__(512, 2) void fused_kernel(Args a) {
  __shared__ __attribute__((aligned(16))) char smem[L_TOT];
  const unsigned lb = (unsigned)(uintptr_t)(LDS3 char*)smem;
  const int xcd = blockIdx.x & 7, idx = blockIdx.x >> 3;
  const int w = __builtin_amdgcn_readfirstlane(threadIdx.x >> 6), lane = threadIdx.x & 63;
  unsigned cpf = 0;
  {
    const char* pc = (const char*)__builtin_amdgcn_s_getpc();
    gld4(cpf, min(w * 64 + lane, CODE_LINES - 1) * 128, pc);
  }
  if (idx >= 28) {
    unsigned acc = 0;
    const int p = (idx - 28) * 8 + w;
#pragma unroll 2
    for (int it = 0; it < 10; ++it) {
      int ln = min((it * 32 + p) * 64 + lane, 18815), qd = ln / 2688, q2 = ln - qd * 2688, kr = q2 / 448, q3 = q2 - kr * 448;
      const unsigned short* src = kr >= 2 ? a.wd + (size_t)(64 * (7 * (kr - 2) + qd)) * DM + q3 * 64
                                          : (kr ? a.cim : a.cre) + (size_t)q3 * DM + qd * 64;
      acc ^= *(const unsigned*)src;
    }
    useu(acc);
    useu(cpf);
    return;
  }
  const int nt = idx % 7, jg = xcd & 1, c = 4 * (xcd >> 1) + idx / 7, j = 7 * jg + nt;
  const int mq = w & 3, nh = w >> 2;
  const int r = lane & 31, h = lane >> 5, o16 = (lane >> 4) & 1, c16 = lane & 15, dist = lane >> 4;
  const char* const wsA = a.ws + OFF_A + (size_t)((4 * c + mq) * KS_ALL) * 1024;
  TS(0);
  unsigned p_re = 0, p_im = 0, p_dt = 0;
  u32x4 raw[2][7];
  if (w == 0) {
    asm volatile("global_load_ushort %0, %1, %2" : "=v"(p_re) : "v"((32 * j + r) * 2), "s"(a.lre) : "memory");
    asm volatile("global_load_ushort %0, %1, %2" : "=v"(p_im) : "v"((32 * j + r) * 2), "s"(a.lim) : "memory");
    asm volatile("global_load_ushort %0, %1, %2" : "=v"(p_dt) : "v"((32 * j + r) * 2), "s"(a.ldt) : "memory");
  }
  int rhog[2];
  const char* const xb = (const char*)a.x + (size_t)(128 * c) * 896;
#pragma unroll
  for (int g = 0; g < 2; ++g) {
    const int rho = 8 * ((8 * nh + 4 * g + mq + 2 * nt + 1) & 15) + (lane >> 3);
    rhog[g] = rho;
    const unsigned vo = ((rho & 96) + tau(rho & 31)) * 896 + (lane & 7) * 16;
    static_for<7>([&](auto I) { gld16<I * 128>(raw[g][I], vo, xb); });
  }
  const unsigned vq = (lane >> 3) * 896 + (((lane & 7) ^ ((4 * w + (lane >> 4)) & 7)) << 4);
  const char* const bb = (const char*)((w & 2) ? a.bim : a.bre) + (32 * j + 8 * (w & 1)) * 896;
  const char* const wb = (const char*)(nh ? a.wu : a.wg) + 256 * j;
  const char* const inbox = a.ws + OFF_AGG + (size_t)(c * 14 + j) * 8192;
  unsigned warm = 0, warm2 = 0;
  if (nh == 1) {
    static_for<5>([&](auto I) {
      constexpr int slab = I < 4 ? I : 6;
      glds1<7 * SLOT + I * 8192>(vq, bb + slab * 128, lb + mq * 1024);
      glds1<7 * SLOT + I * 8192 + 4096>(vq, bb + slab * 128 + 16 * 896, lb + mq * 1024);
    });
  }
  TS(12);
#pragma unroll
  for (int g = 0; g < 2; ++g) {
    if (nh == 0) { if (g == 0) wait_vm<7>(); else wait_vm<0>(); }
    if (g == 0) wait7<17>(raw[0]); else wait7<10>(raw[1]);
    if (g == 0) { TS(1); } else { TS(14); }
    float s1a = 0.f, s2a = 0.f, s1b = 0.f, s2b = 0.f;
#pragma unroll
    for (int i = 0; i < 7; ++i) {
      s1a = dot2(raw[g][i][0], 0x3f803f80u, s1a); s2a = dot2(raw[g][i][0], raw[g][i][0], s2a);
      s1b = dot2(raw[g][i][1], 0x3f803f80u, s1b); s2b = dot2(raw[g][i][1], raw[g][i][1], s2b);
      s1a = dot2(raw[g][i][2], 0x3f803f80u, s1a); s2a = dot2(raw[g][i][2], raw[g][i][2], s2a);
      s1b = dot2(raw[g][i][3], 0x3f803f80u, s1b); s2b = dot2(raw[g][i][3], raw[g][i][3], s2b);
    }
    float s1 = s1a + s1b, s2 = s2a + s2b;
    s1 = dpp_add<0xB1>(s1); s2 = dpp_add<0xB1>(s2);
    s1 = dpp_add<0x4E>(s1); s2 = dpp_add<0x4E>(s2);
    s1 = dpp_add<0x141>(s1); s2 = dpp_add<0x141>(s2);
    const float mean = s1 * (1.0f / DM);
    const float rstd = __builtin_amdgcn_rsqf(fmaxf(s2 * (1.0f / DM) - mean * mean, 0.f) + 1e-5f);
    const float nb = -mean * rstd;
    const int rho = rhog[g];
    const unsigned wa = lb + rho * 128 + (((lane & 7) ^ ((rho >> 1) & 7)) << 4);
    const f32x2 r2 = {rstd, rstd}, n2 = {nb, nb};
#pragma unroll
    for (int i = 0; i < 7; ++i) {
      f32x2 t[4];
#pragma unroll
      for (int e = 0; e < 4; ++e) t[e] = f32x2{__uint_as_float(raw[g][i][e] << 16), __uint_as_float(raw[g][i][e] & 0xffff0000u)};
      __builtin_amdgcn_sched_barrier(0);
#pragma unroll
      for (int e = 0; e < 4; ++e) t[e] = __builtin_elementwise_fma(t[e], r2, n2);
      __builtin_amdgcn_sched_barrier(0);
      u32x4 ov;
#pragma unroll
      for (int e = 0; e < 4; ++e) ov[e] = pack2(t[e][0], t[e][1]);
      ldsw(wa + i * SLOT, ov);
    }
    ldsw(lb + L_RS + rho * 8, f32x2{rstd, nb});
  }
  useu(cpf);
  TS(2);
  {
    int ln = min(mq * 64 + lane, 223) * 4 + (c & 3), un = ln >= 448, row = ln - 448 * un;
    asm volatile("global_load_dword %0, %1, %2" : "=v"(warm) : "v"(row * 3584 + un * 128), "s"(wb) : "memory");
  }
  if (w == 0) {
    asm volatile("" : "+v"(p_re), "+v"(p_im), "+v"(p_dt));
    float dt = expf(bf2f(p_dt)), are = bf2f(p_re), aim = bf2f(p_im);
    float e = expf(dt * are), sn, cn;
    sincosf(dt * aim, &sn, &cn);
    float lr = e * cn, li = e * sn;
    float inv = 1.0f / (are * are + aim * aim);
    float nr = lr - 1.0f, ni = li;
    f32x4 pv = {lr, li, (nr * are + ni * aim) * inv, (ni * are - nr * aim) * inv};
    ldsw(lb + L_PAR + r * 16, pv);
  }
  TS(13);
  const unsigned lw1 = lb + w * 1024, lw2 = lb + w * 2048;
  const char* const bb2 = bb + nh * (16 * 896);
  const unsigned vw0 = (16 * mq + (lane >> 3)) * 3584 + (((lane & 7) ^ (2 * (2 * nh + ((lane >> 4) & 1)))) << 4);
  const unsigned vw1 = vw0 + 8 * 3584 - 1024;
  auto stage_w_ = [&](auto N) {
    constexpr int n = N;
    glds2<wslot(n) * SLOT>(vw0, vw1, wb + (n / 7) * 128 + (n % 7) * (64 * 3584), lw2);
  };
  const char* pd = (const char*)a.wd + nt * 128 + w * (8 * 896);
  const char* pc0 = (const char*)a.cre + (64 * nt + 8 * w) * 896;
  const char* pc1 = (const char*)a.cim + (64 * nt + 8 * w) * 896;
  asm volatile("" : "+s"(pd), "+s"(pc0), "+s"(pc1));
  auto stage_u = [&](auto U) {
    constexpr int u = U, kr = u / 3, qd = u % 3;
    glds1<u * 8192>(vq, kr >= 2 ? pd + (64 * (7 * (kr - 2) + qd)) * 896 : (kr ? pc1 : pc0) + qd * 128, lw1);
  };
#define stage_w(n) stage_w_(IC(n))
#define stage_p(sl) { stage_u(IC(2 * (sl))); stage_u(IC(2 * (sl) + 1)); }

  unsigned toff[4];
#pragma unroll
  for (int ks = 0; ks < 4; ++ks) toff[ks] = lb + r * 128 + (((2 * ks + h) ^ ((r >> 1) & 7)) << 4);
  const int q4 = (lane >> 2) & 3;
  unsigned wta[2][3];
#pragma unroll
  for (int t = 0; t < 2; ++t) {
    wta[t][0] = lb + o16 * 8192 + (8 * h + q4) * 128 + (((2 * nh + t) ^ (2 * o16 + (q4 >> 1))) << 5) + (lane & 3) * 8;
    wta[t][1] = wta[t][0] + 3 * SLOT; wta[t][2] = wta[t][0] + 6 * SLOT;
    keepu(wta[t][1]); keepu(wta[t][2]);
  }

  {
  bf16x8 xf[28];
  bf16x8 fs[4], fr[3][2];
  f32x16 accs = {}, accu[2][2] = {};
  float lr, li, cfr, cfi, l64r, l64i, lcr, lci, l2r, l2i, l4r, l4i, yr = 0.f, yi = 0.f, t0r, t0i, t1r, t1i;
  float l8r, l8i, l16r, l16i, l32r, l32i, clr = 0.f, cli = 0.f, er = 0.f, ei = 0.f;
  float ur[8], ui[8];
  u32x4 gr[4], ov[2];
  float pbr, pbi;
  const unsigned segb = lb + L_SEG + nh * 512 + c16 * 8;
  f32x2 sg[4];
  const char* const pubb = a.ws + OFF_AGG + (4 * mq * 14 + j) * 8192 + c * 512 + nh * 256;
  auto publish = [&]() {
    int l2 = lane; keepi(l2);
    st16((l2 >> 4) * (14 * 8192) + (l2 & 15) * 16, pubb, u32x4{__float_as_uint(pbr), 1u, __float_as_uint(pbi), 1u});
  };

  auto load_x = [&](auto KI) {
    constexpr int k = KI;
    xf[k] = lds_b128(toff[k & 3] + mq * 4096 + (k >> 2) * SLOT);
  };
  auto load_s = [&](auto SI) {
    constexpr int s = SI, k = kstep_of(s), sl = k >> 2;
    constexpr int off = sl < 4 ? 7 * SLOT + sl * 8192 : (sl == 6 ? L_D3 : (sl - 4) * 8192);
    fs[s & 3] = lds_b128(toff[k & 3] + nh * 4096 + off);
  };
  auto load_w = [&](auto SI, int t) {
    constexpr int s = SI;
    if constexpr (s < 84) {
      constexpr int n = (s - 28) >> 2, ks = s & 3, sl = wslot(n);
      unsigned ad = wta[t][sl / 3] + (sl % 3) * SLOT + ks * 2048;
      fr[s % 3][t] = lds_tr(ad, ad + 512);
    }
  };
  auto clear_inbox = [&]() {
    u32x4 z = {0u, 0u, 0u, 0u};
    st16(w == 6 && lane < 8 ? (unsigned)(OFF_FLG - OFF_AGG) + lane * 16 - (c * 14 + j) * 8064 : w * 1024 + lane * 16, inbox, z);
    st16(w * 1024 + lane * 16, inbox, z);
  };
  auto poll = [&]() {
    int l2 = lane; keepi(l2);
#pragma unroll
    for (int n = 0; n < 4; ++n) gld16_sc1(gr[n], ((c - 1 - 4 * n - (l2 >> 4)) & 15) * 512 + (l2 & 15) * 16, inbox + nh * 256);
  };
  auto act1 = [&](float g, float u) { return (g * u) * __builtin_amdgcn_rcpf(1.0f + __builtin_amdgcn_exp2f(g * -1.44269504f)); };
  auto epi2 = [&](const f32x16& ac, int i) { return pack2(act1(ac[2 * i], ac[8 + 2 * i]), act1(ac[2 * i + 1], ac[9 + 2 * i])); };

#define AT(x, q0, stmt) if constexpr (x == (q0)) { stmt; }
  const char* sta = wsA + (56 + 8 * j + 2 * nh + 3) * 1024;
  const char* sth = wsA + (2 * j + nh) * 1024;
  asm volatile("" : "+s"(sta), "+s"(sth));
  unsigned hw0, hw1;
  auto hook = [&](auto QI) {
    constexpr int q = QI;
    AT(q, 0, stage_w(5)) AT(q, 11, stage_w(6)) AT(q, 13, stage_w(7)) AT(q, 22, stage_w(8)) AT(q, 26, stage_w(9))
    AT(q, 28, stage_w(10)) AT(q, 33, stage_w(11)) AT(q, 36, poll()) AT(q, 38, stage_w(12)) AT(q, 44, stage_w(13))
    AT(q, 60, stage_p(7)) AT(q, 65, stage_p(8)) AT(q, 77, stage_p(0)) AT(q, 81, stage_p(1)) AT(q, 87, stage_p(6))
    AT(q, 93, stage_p(2)) AT(q, 97, stage_p(3)) AT(q, 108, stage_p(4)) AT(q, 109, stage_p(5))
    if constexpr (q >= 1 && q < 9) {
      constexpr int i = q - 1;
      auto sw = __builtin_amdgcn_permlane16_swap(__float_as_uint(accs[i]), __float_as_uint(accs[i + 8]), false, false);
      cmul(ur[i], ui[i], cfr, cfi, __uint_as_float(sw[0]), __uint_as_float(sw[1]));
      if constexpr (i == 0) { er = ur[0]; ei = ui[0]; } else cfma(er, ei, lr, li, er, ei, ur[i], ui[i]);
    }
    if constexpr (q == 9) { swap16(er, t0r, yr); swap16(ei, t0i, yi); cfma(er, ei, l8r, l8i, t0r, t0i, yr, yi); }
    if constexpr (q == 10) {
      swap32(er, t1r, yr); swap32(ei, t1i, yi);
      cfma(yr, yi, l16r, l16i, t1r, t1i, yr, yi);
      ldsw(segb + mq * 128, f32x2{yr, yi});
    }
    if constexpr (q == 12) {
#pragma unroll
      for (int i = 0; i < 4; ++i) sg[i] = ldsr<f32x2>(segb + i * 128);
      t1r = h ? t1r : 0.f; t1i = h ? t1i : 0.f; cfma(yr, yi, l8r, l8i, t1r, t1i, t0r, t0i);
    }
    if constexpr (q == 13) { clr = o16 ? yr : t1r; cli = o16 ? yi : t1i; pin2(clr, cli); }
    if constexpr (q == 14) cfma(t0r, t0i, l32r, l32i, sg[0][0], sg[0][1], sg[1][0], sg[1][1]);
    if constexpr (q == 15) cfma(t1r, t1i, l32r, l32i, t0r, t0i, sg[2][0], sg[2][1]);
    if constexpr (q == 16) cfma(pbr, pbi, l32r, l32i, t1r, t1i, sg[3][0], sg[3][1]);
    if constexpr (q == 17) publish();
    if constexpr (q == 18) {
      er = mq == 0 ? 0.f : (mq == 1 ? sg[0][0] : (mq == 2 ? t0r : t1r));
      ei = mq == 0 ? 0.f : (mq == 1 ? sg[0][1] : (mq == 2 ? t0i : t1i));
      pin2(er, ei);
    }
    if constexpr (q == 19) { yr = h ? l16r : 1.f; yi = h ? l16i : 0.f; t1r = o16 ? l8r : 1.f; t1i = o16 ? l8i : 0.f; pin2(yr, yi); pin2(t1r, t1i); }
    if constexpr (q == 20) cmul(yr, yi, yr, yi, t1r, t1i);
    if constexpr (q == 21) cfma(clr, cli, yr, yi, er, ei, clr, cli);
    if constexpr (q == 23) csq(l32r, l32i, l64r, l64i);
    if constexpr (q == 24) { t1r = (mq & 1) ? l32r : 1.f; t1i = (mq & 1) ? l32i : 0.f; cmul(yr, yi, yr, yi, t1r, t1i); }
    if constexpr (q == 25) { t0r = (mq & 2) ? l64r : 1.f; t0i = (mq & 2) ? l64i : 0.f; cmul(t0r, t0i, t0r, t0i, yr, yi); }
    if constexpr (q == 30) csq(l64r, l64i, lcr, lci);
    if constexpr (q == 31) csq(lcr, lci, l2r, l2i);
    if constexpr (q == 32) csq(l2r, l2i, l4r, l4i);
    if constexpr (q == 45) {
      wait4<4>(gr);
      for (int spin = 0; spin < SPIN; ++spin) {
        unsigned ok = (gr[0][1] & gr[0][3] & gr[1][1]) & (gr[1][3] & gr[2][1] & gr[2][3]) & (gr[3][1] & gr[3][3]);
        if (__all(ok != 0u)) break;
        __builtin_amdgcn_s_sleep(1);
        poll();
        wait4<0>(gr);
      }
      TS(29);
    }
    if constexpr (q == 46) { keepv(pbr); publish(); }
    if constexpr (q >= 47 && q < 51) {
      constexpr int n = 50 - q;
      bool v = 4 * n + dist < c;
      float xr = v ? __uint_as_float(gr[n][0]) : 0.f, xi = v ? __uint_as_float(gr[n][2]) : 0.f;
      if constexpr (n == 3) { yr = xr; yi = xi; pin2(yr, yi); }
      else cfma(yr, yi, l4r, l4i, yr, yi, xr, xi);
    }
    if constexpr (q == 51) { swap16(yr, er, t1r); swap16(yi, ei, t1i); pin2(er, t1r); pin2(ei, t1i); }
    if constexpr (q == 52) cfma(yr, yi, lcr, lci, t1r, t1i, er, ei);
    if constexpr (q == 53) { swap32(yr, er, t1r); swap32(yi, ei, t1i); pin2(er, t1r); pin2(ei, t1i); }
    if constexpr (q == 54) { cfma(yr, yi, l2r, l2i, t1r, t1i, er, ei); cfma(er, ei, t0r, t0i, yr, yi, clr, cli); }
    if constexpr (q == 55) {
      hw0 = lb + 6 * SLOT + w * 2048 + (16 * o16 + 4 * h) * 64 + ((c16 * 4) ^ (h << 4));
      hw1 = (hw0 ^ 32) + 512;
      keepu(hw0); keepu(hw1);
    }
    if constexpr (q >= 56 && q < 64) {
      constexpr int i = q - 56;
      cfma(er, ei, lr, li, er, ei, ur[i], ui[i]);
      ldsw((i < 4 ? hw0 : hw1) + (i & 3) * 64, pack2(er, ei));
    }
    if constexpr (q == 64) {
      cbar();
#pragma unroll
      for (int e = 0; e < 2; ++e) ov[e] = ldsr<u32x4>(lb + 6 * SLOT + w * 2048 + r * 64 + (((2 * h + e) ^ ((r >> 2) & 3)) << 4));
    }
    if constexpr (q == 66 || q == 67) {
      u32x4 o;
#pragma unroll
      for (int i = 0; i < 4; ++i)
        o[i] = __builtin_amdgcn_perm(ov[i >> 1][2 * (i & 1) + 1], ov[i >> 1][2 * (i & 1)], q == 66 ? 0x05040100u : 0x07060302u);
      st16o<0>(lane * 16, sth + (q - 66) * 28 * 1024, o);
    }
    if constexpr (q >= 68 && q < 100 && (q & 1) == 0) {
      constexpr int e = (q - 68) >> 1, t = e >> 3, i = e & 7;
      if constexpr ((i & 1) == 0) { t1r = act1(accu[0][t][i], accu[0][t][8 + i]); keepv(t1r); }
      else { unsigned pk = pack2(t1r, act1(accu[0][t][i], accu[0][t][8 + i])); keepu(pk); ov[t][i >> 1] = pk; }
    }
    AT(q, 83, st16o<-3072>(lane * 16, sta, ov[0])) AT(q, 99, st16o<-2048>(lane * 16, sta, ov[1]))
  };

  bar_vm<1>();
  TS(3);
  static_for<4>(load_x);
  static_for<3>(load_s);

  static_for<84>([&](auto SI) {
    constexpr int s = SI;
    if constexpr (s == 3 || s == 6) { bar_lgkm(); TS(s == 3 ? 4 : 5); }
    if constexpr (s == 17) { bar_vm<6>(); useu(warm); TS(6); }
    if constexpr (s == 26) { bar_vm<6>(); TS(7); }
    if constexpr (s == 34) { bar_vm<6>(); TS(8); }
    if constexpr (s == 42) { bar_vm<9>(); TS(9); }
    if constexpr (s == 50) { bar_vm<15>(); TS(10); }
    if constexpr (s == 58) { bar_vm<1>(); TS(11); }
    if constexpr (s == 66 || s == 74 || s == 82) { bar_lgkm(); TS(26 + (s - 66) / 8); }
    if constexpr (s < 28) {
      if constexpr (s < 6) static_for<4>([&](auto I) { load_x(std::integral_constant<int, 4 * (s + 1) + I>{}); });
      if constexpr (s + 3 < 28) load_s(std::integral_constant<int, s + 3>{});
      if constexpr (s >= 26) { load_w(std::integral_constant<int, s + 2>{}, 0); load_w(std::integral_constant<int, s + 2>{}, 1); }
      __builtin_amdgcn_sched_barrier(0);
      accs = mfma(xf[kstep_of(s)], fs[s & 3], accs);
      __builtin_amdgcn_sched_barrier(0);
      AT(s, 3, glds1<0>(vq, bb2 + 512, lw1); glds1<8192>(vq, bb2 + 640, lw1))
      AT(s, 6, stage_w(0)) AT(s, 10, stage_w(1))
      AT(s, 13, clear_inbox())
      AT(s, 17, stage_w(2)) AT(s, 21, stage_w(3)) AT(s, 26, stage_w(4))
      if constexpr (s == 16) {
        f32x4 pv = ldsr<f32x4>(lb + L_PAR + (16 * nh + c16) * 16);
        lr = pv[0]; li = pv[1]; cfr = pv[2]; cfi = pv[3];
      }
      if constexpr (s == 18) csq(lr, li, t0r, t0i);
      if constexpr (s == 20) csq(t0r, t0i, t1r, t1i);
      if constexpr (s == 22) csq(t1r, t1i, l8r, l8i);
      if constexpr (s == 23) csq(l8r, l8i, l16r, l16i);
      if constexpr (s == 25) csq(l16r, l16i, l32r, l32i);
      __builtin_amdgcn_sched_barrier(0);
    } else {
      constexpr int u = (s - 28) / 28;
      accu[u][0] = mfma(fr[s % 3][0], xf[kstep_of(s)], accu[u][0]);
      __builtin_amdgcn_sched_barrier(0);
      load_w(std::integral_constant<int, s + 2>{}, 0);
      hook(std::integral_constant<int, 2 * (s - 28)>{});
      __builtin_amdgcn_sched_barrier(0);
      accu[u][1] = mfma(fr[s % 3][1], xf[kstep_of(s)], accu[u][1]);
      __builtin_amdgcn_sched_barrier(0);
      load_w(std::integral_constant<int, s + 2>{}, 1);
      hook(std::integral_constant<int, 2 * (s - 28) + 1>{});
      __builtin_amdgcn_sched_barrier(0);
    }
  });
  TS(15);
#pragma unroll
  for (int t = 0; t < 2; ++t) {
#pragma unroll
    for (int i = 0; i < 4; ++i) ov[t][i] = epi2(accu[1][t], i);
    if (t == 0) st16o<1024>(lane * 16, sta, ov[t]); else st16o<2048>(lane * 16, sta, ov[t]);
  }
  }
  TS(16);
  bar_vm<0>();
  TS(17);

  int tid = threadIdx.x;
  asm volatile("" : "+v"(tid));
  const int c4 = tid & 15;
  u32x2 xv[2];
  const u32x2 dk = *(const u32x2*)(a.dsk + 64 * nt + 4 * c4);
  int trow[2];
#pragma unroll
  for (int e = 0; e < 2; ++e) {
    trow[e] = 128 * c + 64 * jg + 32 * e + tau(tid >> 4);
    xv[e] = *(const u32x2*)(a.x + (size_t)trow[e] * DM + 64 * nt + 4 * c4);
  }
  if (w < 6) {
    const int lane = tid & 63, r = lane & 31, h = lane >> 5, o16 = (lane >> 4) & 1, q4 = (lane >> 2) & 3;
    unsigned toff[4];
#pragma unroll
    for (int ks = 0; ks < 4; ++ks) toff[ks] = lb + r * 128 + (((2 * ks + h) ^ ((r >> 1) & 7)) << 4);
    bf16x8 bq[3][4][2];
    u32x4 af[PA][2];
    f32x16 acc[2][2] = {};
    const unsigned rg = w * 24576;
    unsigned tra[2][2];
#pragma unroll
    for (int n = 0; n < 2; ++n)
#pragma unroll
      for (int e = 0; e < 2; ++e)
        tra[n][e] = lb + rg + (4 * h + q4 + 8 * e) * 128 + (((4 * n + 2 * o16 + ((lane >> 1) & 1)) ^ ((2 * h + (q4 >> 1) + 4 * e) & 7)) << 4) + (lane & 1) * 8;
    auto read_quad = [&](int u) {
      if (w < 2) {
#pragma unroll
        for (int i = 0; i < 4; ++i)
#pragma unroll
          for (int n = 0; n < 2; ++n) bq[u][i][n] = lds_b128(toff[i] + rg + u * 8192 + n * 4096);
      } else {
#pragma unroll
        for (int i = 0; i < 4; ++i)
#pragma unroll
          for (int n = 0; n < 2; ++n) bq[u][i][n] = lds_tr(tra[n][0] + u * 8192 + i * 2048, tra[n][1] + u * 8192 + i * 2048);
      }
    };
    const unsigned vq0 = (lane >> 3) * 896 + (((lane & 7) ^ (lane >> 4)) << 4);
    const unsigned vq1 = ((lane >> 3) + 8) * 896 + (((lane & 7) ^ (4 + (lane >> 4))) << 4) - 1024;
    const char* const qsrc = w >= 2 ? (const char*)a.wd + (size_t)(448 * (w - 2)) * 896 + nt * 128
                                    : (const char*)(w ? a.cim : a.cre) + (64 * nt) * 896;
    const int qstr = w >= 2 ? 64 * 896 : 128;
    auto stage_quad = [&](auto QD) {
      constexpr int qd = QD;
      static_for<4>([&](auto I) { glds2<(qd % 3) * 8192 + I * 2048>(vq0, vq1, qsrc + qd * qstr + I * (16 * 896), lb + rg); });
    };
    read_quad(0); read_quad(1); read_quad(2);
    asm volatile("s_waitcnt lgkmcnt(0)" ::: "memory");
    __builtin_amdgcn_sched_barrier(0);
    stage_quad(IC(3));
    TS(20);
    __builtin_amdgcn_s_barrier();
    TS(21);
    const char* const pa = a.ws + OFF_A + (size_t)((4 * c + 2 * jg) * KS_ALL + 28 * w) * 1024;
    const unsigned l0 = lane * 16, l1 = lane * 16 + KS_ALL * 1024;
    auto lda = [&](int s) {
      if (ACQ) { gld16<0>(af[s % PA][0], l0, pa + s * 1024); gld16<0>(af[s % PA][1], l1, pa + s * 1024); }
      else { gld16_sc1(af[s % PA][0], l0, pa + s * 1024); gld16_sc1(af[s % PA][1], l1, pa + s * 1024); }
    };
#pragma unroll
    for (int s = 0; s < PA; ++s) lda(s);
    static_for<28>([&](auto SI) {
      constexpr int s = SI;
      wait2<young2(s)>(af[s % PA][0], af[s % PA][1]);
#pragma unroll
      for (int m = 0; m < 2; ++m)
#pragma unroll
        for (int n = 0; n < 2; ++n)
          acc[m][n] = mfma(bq[(s >> 2) % 3][s & 3][n], __builtin_bit_cast(bf16x8, af[s % PA][m]), acc[m][n]);
      __builtin_amdgcn_sched_barrier(0);
      if constexpr (s + PA < 28) lda(s + PA);
      if constexpr (s == 8) wait_lgkm0();
      if constexpr (s < 12) {
        constexpr int qd = 4 + (s >> 2), i = s & 3;
        glds2<(qd % 3) * 8192 + i * 2048>(vq0, vq1, qsrc + qd * qstr + i * (16 * 896), lb + rg);
      }
      if constexpr (s == 3) read_quad(0);
      if constexpr (s == 11 || s == 15 || s == 19) read_quad(((s - 7) >> 2) % 3);
      if constexpr (s == 0) { TS(22); }
      __builtin_amdgcn_sched_barrier(0);
    });
    TS(23);
    const unsigned pw = lb + rg + r * 272 + h * 16;
#pragma unroll
    for (int m = 0; m < 2; ++m)
#pragma unroll
      for (int n = 0; n < 2; ++n)
#pragma unroll
        for (int g = 0; g < 4; ++g) {
          f32x4 v = {acc[m][n][4 * g], acc[m][n][4 * g + 1], acc[m][n][4 * g + 2], acc[m][n][4 * g + 3]};
          ldsw(pw + m * 32 * 272 + n * 128 + g * 32, v);
        }
  } else {
    if (w == 6) {
      const int ln = min(lane, 13);
      GLB1 unsigned* const fo = (GLB1 unsigned*)(a.ws + OFF_FLG + (c * 14 + ln) * 128 + j * 4);
      GLB1 unsigned* const fi = (GLB1 unsigned*)(a.ws + OFF_FLG + (c * 14 + j) * 128 + ln * 4);
      __hip_atomic_store(fo, 1u, __ATOMIC_RELAXED, __HIP_MEMORY_SCOPE_AGENT);
      for (int spin = 0; spin < SPIN; ++spin) {
        unsigned v = __hip_atomic_load(fi, __ATOMIC_RELAXED, __HIP_MEMORY_SCOPE_AGENT);
        if (__all(v == 1u)) break;
        __builtin_amdgcn_s_sleep(1);
      }
      TS(20);
      if (ACQ) {
        __builtin_amdgcn_fence(__ATOMIC_ACQUIRE, "agent");
        asm volatile("s_waitcnt vmcnt(0)" ::: "memory");
      }
      asm volatile("s_barrier" ::: "memory");
      __hip_atomic_store(fo, 1u, __ATOMIC_RELAXED, __HIP_MEMORY_SCOPE_AGENT);
    } else {
      TS(20);
      asm volatile("s_barrier" ::: "memory");
    }
    TS(21); TS(22); TS(23);
  }
  bar_lgkm();
  TS(24);
#pragma unroll
  for (int e = 0; e < 2; ++e) {
    const int row = (tid >> 4) + 32 * e;
    const unsigned pr = lb + row * 272 + c4 * 16;
    f32x4 sv = ldsr<f32x4>(pr) - ldsr<f32x4>(pr + 24576);
    f32x4 dv = (ldsr<f32x4>(pr + 2 * 24576) + ldsr<f32x4>(pr + 3 * 24576)) + (ldsr<f32x4>(pr + 4 * 24576) + ldsr<f32x4>(pr + 5 * 24576));
    const f32x2 st = ldsr<f32x2>(lb + L_RS + (64 * jg + row) * 8);
    f32x4 o;
#pragma unroll
    for (int i = 0; i < 4; ++i) {
      unsigned xw = xv[e][i >> 1], dw = dk[i >> 1];
      float xf = __uint_as_float((i & 1) ? (xw & 0xffff0000u) : (xw << 16));
      float df = __uint_as_float((i & 1) ? (dw & 0xffff0000u) : (dw << 16));
      o[i] = xf + (2.0f * sv[i] + fmaf(xf, st[0], st[1]) * df) * silu(dv[i]);
    }
    st16((trow[e] * DM + 64 * nt + 4 * c4) * 4, a.out, __builtin_bit_cast(u32x4, o));
  }
}

extern "C" void kernel_launch(void* const* d_in, const int* in_sizes, int n_in, void* d_out, int out_size,
                              void* d_ws, size_t ws_size, hipStream_t stream) {
  Args a;
  a.x = (const unsigned short*)d_in[0];
  a.lre = (const unsigned short*)d_in[1];
  a.lim = (const unsigned short*)d_in[2];
  a.bre = (const unsigned short*)d_in[3];
  a.bim = (const unsigned short*)d_in[4];
  a.cre = (const unsigned short*)d_in[5];
  a.cim = (const unsigned short*)d_in[6];
  a.ldt = (const unsigned short*)d_in[7];
  a.dsk = (const unsigned short*)d_in[8];
  a.wg = (const unsigned short*)d_in[10];
  a.wu = (const unsigned short*)d_in[11];
  a.wd = (const unsigned short*)d_in[12];
  a.ws = (char*)d_ws;
  a.out = (float*)d_out;
  fused_kernel<<<256, 512, 0, stream>>>(a);
}
```

```cpp
#include <hip/hip_runtime.h>
#include <hip/hip_bf16.h>
#include <stdint.h>
#include <utility>

typedef __attribute__((ext_vector_type(8))) __bf16 bf16x8;
typedef __attribute__((ext_vector_type(4))) __bf16 bf16x4;
typedef __attribute__((ext_vector_type(2))) __bf16 bf16x2;
typedef __attribute__((ext_vector_type(16))) float f32x16;
typedef __attribute__((ext_vector_type(4))) float f32x4;
typedef __attribute__((ext_vector_type(2))) float f32x2;
typedef __attribute__((ext_vector_type(4))) unsigned u32x4;
typedef __attribute__((ext_vector_type(2))) unsigned u32x2;
typedef unsigned long long u64;
#define LDS3 __attribute__((address_space(3)))
#define GLB1 __attribute__((address_space(1)))
#define DEVI __device__ __forceinline__

constexpr int DM = 448, FF = 1792;
constexpr int KS_ALL = 168;
constexpr size_t OFF_AGG = 0;
constexpr size_t OFF_FLG = 224 * 8192;
constexpr size_t OFF_A = 2097152;

template <int... Is, class Fn>
DEVI void static_for_impl(std::integer_sequence<int, Is...>, Fn&& f) {
  (f(std::integral_constant<int, Is>{}), ...);
}
template <int N, class Fn>
DEVI void static_for(Fn&& f) { static_for_impl(std::make_integer_sequence<int, N>{}, f); }

template <int OFF> DEVI void glds2(unsigned v0, unsigned v1, const void* sbase, unsigned base) {
  asm volatile("s_add_i32 m0, %3, %4\n\ts_nop 0\n\tglobal_load_lds_dwordx4 %0, %2\n\tglobal_load_lds_dwordx4 %1, %2 offset:1024"
               ::"v"(v0), "v"(v1), "s"(sbase), "s"(base), "n"(OFF) : "memory", "scc");
}
template <int OFF> DEVI void glds1(unsigned v0, const void* sbase, unsigned base) {
  asm volatile("s_add_i32 m0, %2, %3\n\ts_nop 0\n\tglobal_load_lds_dwordx4 %0, %1"
               ::"v"(v0), "s"(sbase), "s"(base), "n"(OFF) : "memory", "scc");
}
#define IC(n) std::integral_constant<int, (n)>{}
DEVI void cfma(float& o_r, float& o_i, float ar, float ai, float br, float bi, float cr, float ci) {
  float x, y;
  asm volatile("v_fma_f32 %0, %2, %4, %6\n\tv_fma_f32 %1, %2, %5, %7\n\tv_fma_f32 %0, -%3, %5, %0\n\tv_fma_f32 %1, %3, %4, %1"
               : "=&v"(x), "=&v"(y) : "v"(ar), "v"(ai), "v"(br), "v"(bi), "v"(cr), "v"(ci));
  o_r = x; o_i = y;
}
DEVI void cmul(float& o_r, float& o_i, float ar, float ai, float br, float bi) {
  float x, y;
  asm volatile("v_mul_f32 %0, %2, %4\n\tv_mul_f32 %1, %2, %5\n\tv_fma_f32 %0, -%3, %5, %0\n\tv_fma_f32 %1, %3, %4, %1"
               : "=&v"(x), "=&v"(y) : "v"(ar), "v"(ai), "v"(br), "v"(bi));
  o_r = x; o_i = y;
}
DEVI void csq(float ar, float ai, float& br, float& bi) { cmul(br, bi, ar, ai, ar, ai); }
DEVI void pin2(float& x, float& y) { asm volatile("" : "+v"(x), "+v"(y)); }
template <int OFF> DEVI void st16o(unsigned vo, const void* sb, u32x4 v) {
  asm volatile("global_store_dwordx4 %0, %1, %2 offset:%3 sc1\n\ts_nop 1" ::"v"(vo), "v"(v), "s"(sb), "n"(OFF) : "memory");
}
DEVI void st16(unsigned vo, const void* sb, u32x4 v) {
  asm volatile("global_store_dwordx4 %0, %1, %2 sc1\n\ts_nop 1" ::"v"(vo), "v"(v), "s"(sb) : "memory");
}
template <int N> DEVI void wait_vm_lgkm0() { asm volatile("s_waitcnt vmcnt(%0) lgkmcnt(0)" ::"n"(N) : "memory"); }
DEVI float bf2f(unsigned short v) { return __uint_as_float((unsigned)v << 16); }
DEVI unsigned pack2(float a, float b) {
  f32x2 p = {a, b};
  return __builtin_bit_cast(unsigned, __builtin_convertvector(p, bf16x2));
}
DEVI float dot2(unsigned a, unsigned b, float c) {
  return __builtin_amdgcn_fdot2_f32_bf16(__builtin_bit_cast(bf16x2, a), __builtin_bit_cast(bf16x2, b), c, false);
}
DEVI bf16x8 lds_b128(unsigned addr) { return *(LDS3 bf16x8*)(uintptr_t)addr; }
DEVI bf16x8 lds_tr(unsigned a0, unsigned a1) {
  bf16x4 a = __builtin_amdgcn_ds_read_tr16_b64_v4bf16((LDS3 bf16x4*)(uintptr_t)a0);
  bf16x4 b = __builtin_amdgcn_ds_read_tr16_b64_v4bf16((LDS3 bf16x4*)(uintptr_t)a1);
  return __builtin_shufflevector(a, b, 0, 1, 2, 3, 4, 5, 6, 7);
}
DEVI f32x16 mfma(bf16x8 a, bf16x8 b, f32x16 c) { return __builtin_amdgcn_mfma_f32_32x32x16_bf16(a, b, c, 0, 0, 0); }
DEVI float silu(float g) { return g * __builtin_amdgcn_rcpf(1.0f + __expf(-g)); }

template <int OFF> DEVI void gld16(u32x4& d, unsigned vo, const void* sb) {
  asm volatile("global_load_dwordx4 %0, %1, %2 offset:%3" : "=v"(d) : "v"(vo), "s"(sb), "n"(OFF) : "memory");
}
#define CODEPF 1
#define CODE_LINES 252
#define STAG 0
#define PRIO 2
#define ACQ 0
#define WARM2 1
DEVI void gld16_sc1(u32x4& d, unsigned vo, const void* sb) {
  asm volatile("global_load_dwordx4 %0, %1, %2 sc1" : "=v"(d) : "v"(vo), "s"(sb) : "memory");
}
template <int N> DEVI void wait4(u32x4 (&g)[4]) {
  asm volatile("s_waitcnt vmcnt(%4)" : "+v"(g[0]), "+v"(g[1]), "+v"(g[2]), "+v"(g[3]) : "n"(N) : "memory");
}
template <int N> DEVI void wait1(u32x4& x) { asm volatile("s_waitcnt vmcnt(%1)" : "+v"(x) : "n"(N) : "memory"); }
template <int N> DEVI void wait2(u32x4& x, u32x4& y) {
  asm volatile("s_waitcnt vmcnt(%2)" : "+v"(x), "+v"(y) : "n"(N) : "memory");
}
template <int N> DEVI void wait7(u32x4 (&g)[7]) {
  asm volatile("s_waitcnt vmcnt(%7)" : "+v"(g[0]), "+v"(g[1]), "+v"(g[2]), "+v"(g[3]), "+v"(g[4]), "+v"(g[5]), "+v"(g[6]) : "n"(N) : "memory");
}
DEVI void keepv(float& v) { asm volatile("" : "+v"(v)); }
DEVI void keepi(int& v) { asm volatile("" : "+v"(v)); }
DEVI void useu(unsigned v) { asm volatile("" ::"v"(v)); }
DEVI void gld4(unsigned& d, unsigned vo, const void* sb) { asm volatile("global_load_dword %0, %1, %2" : "=v"(d) : "v"(vo), "s"(sb) : "memory"); }
DEVI void keepu(unsigned& v) { asm volatile("" : "+v"(v)); }
DEVI void wait_lgkm0() { asm volatile("s_waitcnt lgkmcnt(0)" ::: "memory"); }
DEVI void cbar() { asm volatile("" ::: "memory"); }
template <int N> DEVI void wait_vm() { asm volatile("s_waitcnt vmcnt(%0)" ::"n"(N) : "memory"); }
DEVI void bar_lgkm() { asm volatile("s_waitcnt lgkmcnt(0)\n\ts_barrier" ::: "memory"); }
template <int N> DEVI void bar_vm() { asm volatile("s_waitcnt vmcnt(%0) lgkmcnt(0)\n\ts_barrier" ::"n"(N) : "memory"); }
template <class T> DEVI T ldsr(unsigned a) { return *(LDS3 T*)(uintptr_t)a; }
template <class T> DEVI void ldsw(unsigned a, T v) { *(LDS3 T*)(uintptr_t)a = v; }
DEVI int tau(int r) { return 16 * ((r >> 2) & 1) + (r & 3) + 4 * (r >> 3); }
template <int C> DEVI float dpp_add(float v) {
  return v + __uint_as_float(__builtin_amdgcn_update_dpp(0u, __float_as_uint(v), C, 0xf, 0xf, true));
}
DEVI void swap16(float v, float& even, float& odd) {
  auto s = __builtin_amdgcn_permlane16_swap(__float_as_uint(v), __float_as_uint(v), false, false);
  even = __uint_as_float(s[0]); odd = __uint_as_float(s[1]);
}
DEVI void swap32(float v, float& lo, float& hi) {
  auto s = __builtin_amdgcn_permlane32_swap(__float_as_uint(v), __float_as_uint(v), false, false);
  lo = __uint_as_float(s[0]); hi = __uint_as_float(s[1]);
}

#define TS(i)

constexpr int SLOT = 16384;
constexpr int L_D3 = 9 * SLOT;
constexpr int L_SEG = L_D3 + 8192;
constexpr int L_PAR = L_SEG + 1024;
constexpr int L_RS = L_PAR + 512;
constexpr int L_TOT = L_RS + 1024;
constexpr int SPIN = 1 << 18;

struct Args {
  const unsigned short *x, *lre, *lim, *bre, *bim, *cre, *cim, *ldt, *dsk, *wg, *wu, *wd;
  char* ws;
  float* out;
};

constexpr int kstep_of(int s) { return s < 16 ? s : (s < 20 ? s + 8 : (s < 28 ? s - 4 : (s - 28) % 28)); }
constexpr int wslot(int n) { return (n + 1) % 9; }
#define F1B 1
#define PHD 8
constexpr int PH = PHD;
constexpr int PA = 6;
constexpr int young2(int s, int pa, int per, int tend) {
  int n = per * ((s + pa - 1 < 27 ? s + pa - 1 : 27) - s);
  for (int t = (s - pa > 0 ? s - pa : 0); t < s; ++t) n += (t >= 8 && t < tend) ? 2 : 0;
  return n;
}

__global__ __launch_bounds__(512, 2) void fused_kernel(Args a) {
  __shared__ __attribute__((aligned(16))) char smem[L_TOT];
  const unsigned lb = (unsigned)(uintptr_t)(LDS3 char*)smem;
  const int xcd = blockIdx.x & 7, idx = blockIdx.x >> 3;
  const int w = __builtin_amdgcn_readfirstlane(threadIdx.x >> 6), lane = threadIdx.x & 63;
  unsigned cpf = 0;
  {
    const char* pc = (const char*)__builtin_amdgcn_s_getpc();
    gld4(cpf, min(w * 64 + lane, CODE_LINES - 1) * 128, pc);
  }
  if (idx >= 28) {
    unsigned acc = 0;
    const int p = (idx - 28) * 8 + w;
#pragma unroll 2
    for (int it = 0; it < 10; ++it) {
      int ln = min((it * 32 + p) * 64 + lane, 18815), qd = ln / 2688, q2 = ln - qd * 2688, kr = q2 / 448, q3 = q2 - kr * 448;
      const unsigned short* src = kr >= 2 ? a.wd + (size_t)(64 * (7 * (kr - 2) + qd)) * DM + q3 * 64
                                          : (kr ? a.cim : a.cre) + (size_t)q3 * DM + qd * 64;
      acc ^= *(const unsigned*)src;
    }
    useu(acc);
    useu(cpf);
    return;
  }
  const int nt = idx % 7, jg = xcd & 1, c = 4 * (xcd >> 1) + idx / 7, j = 7 * jg + nt;
  const int mq = w & 3, nh = w >> 2;
  const int r = lane & 31, h = lane >> 5, o16 = (lane >> 4) & 1, c16 = lane & 15, dist = lane >> 4;
  const char* const wsA = a.ws + OFF_A + (size_t)((4 * c + mq) * KS_ALL) * 1024;
  TS(0);
  unsigned p_re = 0, p_im = 0, p_dt = 0;
  u32x4 raw[2][7];
  if (w == 0) {
    asm volatile("global_load_ushort %0, %1, %2" : "=v"(p_re) : "v"((32 * j + r) * 2), "s"(a.lre) : "memory");
    asm volatile("global_load_ushort %0, %1, %2" : "=v"(p_im) : "v"((32 * j + r) * 2), "s"(a.lim) : "memory");
    asm volatile("global_load_ushort %0, %1, %2" : "=v"(p_dt) : "v"((32 * j + r) * 2), "s"(a.ldt) : "memory");
  }
  int rhog[2];
  const char* const xb = (const char*)a.x + (size_t)(128 * c) * 896;
#pragma unroll
  for (int g = 0; g < 2; ++g) {
    const int rho = 8 * ((8 * nh + 4 * g + mq + 2 * nt + 1) & 15) + (lane >> 3);
    rhog[g] = rho;
    const unsigned vo = ((rho & 96) + tau(rho & 31)) * 896 + (lane & 7) * 16;
    static_for<7>([&](auto I) { gld16<I * 128>(raw[g][I], vo, xb); });
  }
  const unsigned vq = (lane >> 3) * 896 + (((lane & 7) ^ ((4 * w + (lane >> 4)) & 7)) << 4);
  const char* const bb = (const char*)((w & 2) ? a.bim : a.bre) + (32 * j + 8 * (w & 1)) * 896;
  const char* const wb = (const char*)(nh ? a.wu : a.wg) + 256 * j;
  const char* const inbox = a.ws + OFF_AGG + (size_t)(c * 14 + j) * 8192;
  unsigned warm = 0, warm2 = 0;
  if (nh == 1) {
    static_for<5>([&](auto I) {
      constexpr int slab = I < 4 ? I : 6;
      glds1<7 * SLOT + I * 8192>(vq, bb + slab * 128, lb + mq * 1024);
      glds1<7 * SLOT + I * 8192 + 4096>(vq, bb + slab * 128 + 16 * 896, lb + mq * 1024);
    });
  }
  TS(12);
#pragma unroll
  for (int g = 0; g < 2; ++g) {
    if (nh == 0) { if (g == 0) wait_vm<7>(); else wait_vm<0>(); }
    if (g == 0) wait7<17>(raw[0]); else wait7<10>(raw[1]);
    if (g == 0) { TS(1); } else { TS(14); }
    float s1a = 0.f, s2a = 0.f, s1b = 0.f, s2b = 0.f;
#pragma unroll
    for (int i = 0; i < 7; ++i) {
      s1a = dot2(raw[g][i][0], 0x3f803f80u, s1a); s2a = dot2(raw[g][i][0], raw[g][i][0], s2a);
      s1b = dot2(raw[g][i][1], 0x3f803f80u, s1b); s2b = dot2(raw[g][i][1], raw[g][i][1], s2b);
      s1a = dot2(raw[g][i][2], 0x3f803f80u, s1a); s2a = dot2(raw[g][i][2], raw[g][i][2], s2a);
      s1b = dot2(raw[g][i][3], 0x3f803f80u, s1b); s2b = dot2(raw[g][i][3], raw[g][i][3], s2b);
    }
    float s1 = s1a + s1b, s2 = s2a + s2b;
    s1 = dpp_add<0xB1>(s1); s2 = dpp_add<0xB1>(s2);
    s1 = dpp_add<0x4E>(s1); s2 = dpp_add<0x4E>(s2);
    s1 = dpp_add<0x141>(s1); s2 = dpp_add<0x141>(s2);
    const float mean = s1 * (1.0f / DM);
    const float rstd = __builtin_amdgcn_rsqf(fmaxf(s2 * (1.0f / DM) - mean * mean, 0.f) + 1e-5f);
    const float nb = -mean * rstd;
    const int rho = rhog[g];
    const unsigned wa = lb + rho * 128 + (((lane & 7) ^ ((rho >> 1) & 7)) << 4);
    const f32x2 r2 = {rstd, rstd}, n2 = {nb, nb};
#pragma unroll
    for (int i = 0; i < 7; ++i) {
      f32x2 t[4];
#pragma unroll
      for (int e = 0; e < 4; ++e) t[e] = f32x2{__uint_as_float(raw[g][i][e] << 16), __uint_as_float(raw[g][i][e] & 0xffff0000u)};
      __builtin_amdgcn_sched_barrier(0);
#pragma unroll
      for (int e = 0; e < 4; ++e) t[e] = __builtin_elementwise_fma(t[e], r2, n2);
      __builtin_amdgcn_sched_barrier(0);
      u32x4 ov;
#pragma unroll
      for (int e = 0; e < 4; ++e) ov[e] = pack2(t[e][0], t[e][1]);
      ldsw(wa + i * SLOT, ov);
    }
    ldsw(lb + L_RS + rho * 8, f32x2{rstd, nb});
  }
  useu(cpf);
  TS(2);
  {
    int ln = min(mq * 64 + lane, 223) * 4 + (c & 3), un = ln >= 448, row = ln - 448 * un;
    asm volatile("global_load_dword %0, %1, %2" : "=v"(warm) : "v"(row * 3584 + un * 128), "s"(wb) : "memory");
  }
  if (w == 0) {
    asm volatile("" : "+v"(p_re), "+v"(p_im), "+v"(p_dt));
    float dt = expf(bf2f(p_dt)), are = bf2f(p_re), aim = bf2f(p_im);
    float e = expf(dt * are), sn, cn;
    sincosf(dt * aim, &sn, &cn);
    float lr = e * cn, li = e * sn;
    float inv = 1.0f / (are * are + aim * aim);
    float nr = lr - 1.0f, ni = li;
    f32x4 pv = {lr, li, (nr * are + ni * aim) * inv, (ni * are - nr * aim) * inv};
    ldsw(lb + L_PAR + r * 16, pv);
  }
  TS(13);
  const unsigned lw1 = lb + w * 1024, lw2 = lb + w * 2048;
  const char* const bb2 = bb + nh * (16 * 896);
  const unsigned vw0 = (16 * mq + (lane >> 3)) * 3584 + (((lane & 7) ^ (2 * (2 * nh + ((lane >> 4) & 1)))) << 4);
  const unsigned vw1 = vw0 + 8 * 3584 - 1024;
  auto stage_w_ = [&](auto N) {
    constexpr int n = N;
    glds2<wslot(n) * SLOT>(vw0, vw1, wb + (n / 7) * 128 + (n % 7) * (64 * 3584), lw2);
  };
  const char* pd = (const char*)a.wd + nt * 128 + w * (8 * 896);
  const char* pc0 = (const char*)a.cre + (64 * nt + 8 * w) * 896;
  const char* pc1 = (const char*)a.cim + (64 * nt + 8 * w) * 896;
  asm volatile("" : "+s"(pd), "+s"(pc0), "+s"(pc1));
  auto stage_u = [&](auto U) {
    constexpr int u = U, kr = u / 3, qd = u % 3;
    glds1<u * 8192>(vq, kr >= 2 ? pd + (64 * (7 * (kr - 2) + qd)) * 896 : (kr ? pc1 : pc0) + qd * 128, lw1);
  };
#define stage_w(n) stage_w_(IC(n))
#define stage_p(sl) { stage_u(IC(2 * (sl))); stage_u(IC(2 * (sl) + 1)); }

  unsigned toff[4];
#pragma unroll
  for (int ks = 0; ks < 4; ++ks) toff[ks] = lb + r * 128 + (((2 * ks + h) ^ ((r >> 1) & 7)) << 4);
  const int q4 = (lane >> 2) & 3;
  unsigned wta[2][3];
#pragma unroll
  for (int t = 0; t < 2; ++t) {
    wta[t][0] = lb + o16 * 8192 + (8 * h + q4) * 128 + (((2 * nh + t) ^ (2 * o16 + (q4 >> 1))) << 5) + (lane & 3) * 8;
    wta[t][1] = wta[t][0] + 3 * SLOT; wta[t][2] = wta[t][0] + 6 * SLOT;
    keepu(wta[t][1]); keepu(wta[t][2]);
  }

  {
  bf16x8 xf[28];
  bf16x8 fs[4], fr[3][2];
  f32x16 accs = {}, accu[2][2] = {};
  float lr, li, cfr, cfi, l64r, l64i, lcr, lci, l2r, l2i, l4r, l4i, yr = 0.f, yi = 0.f, t0r, t0i, t1r, t1i;
  float l8r, l8i, l16r, l16i, l32r, l32i, clr = 0.f, cli = 0.f, er = 0.f, ei = 0.f;
  float ur[8], ui[8];
  u32x4 gr[4], ov[2];
  float pbr, pbi;
  const unsigned segb = lb + L_SEG + nh * 512 + c16 * 8;
  f32x2 sg[4];
  const char* const pubb = a.ws + OFF_AGG + (4 * mq * 14 + j) * 8192 + c * 512 + nh * 256;
  auto publish = [&]() {
    int l2 = lane; keepi(l2);
    st16((l2 >> 4) * (14 * 8192) + (l2 & 15) * 16, pubb, u32x4{__float_as_uint(pbr), 1u, __float_as_uint(pbi), 1u});
  };

  auto load_x = [&](auto KI) {
    constexpr int k = KI;
    xf[k] = lds_b128(toff[k & 3] + mq * 4096 + (k >> 2) * SLOT);
  };
  auto load_s = [&](auto SI) {
    constexpr int s = SI, k = kstep_of(s), sl = k >> 2;
    constexpr int off = sl < 4 ? 7 * SLOT + sl * 8192 : (sl == 6 ? L_D3 : (sl - 4) * 8192);
    fs[s & 3] = lds_b128(toff[k & 3] + nh * 4096 + off);
  };
  auto load_w = [&](auto SI, int t) {
    constexpr int s = SI;
    if constexpr (s < 84) {
      constexpr int n = (s - 28) >> 2, ks = s & 3, sl = wslot(n);
      unsigned ad = wta[t][sl / 3] + (sl % 3) * SLOT + ks * 2048;
      fr[s % 3][t] = lds_tr(ad, ad + 512);
    }
  };
  auto clear_inbox = [&]() {
    u32x4 z = {0u, 0u, 0u, 0u};
    st16(w == 6 && lane < 8 ? (unsigned)(OFF_FLG - OFF_AGG) + lane * 16 - (c * 14 + j) * 8064 : w * 1024 + lane * 16, inbox, z);
    st16(w * 1024 + lane * 16, inbox, z);
  };
  auto poll = [&]() {
    int l2 = lane; keepi(l2);
#pragma unroll
    for (int n = 0; n < 4; ++n) gld16_sc1(gr[n], ((c - 1 - 4 * n - (l2 >> 4)) & 15) * 512 + (l2 & 15) * 16, inbox + nh * 256);
  };
  auto act1 = [&](float g, float u) { return (g * u) * __builtin_amdgcn_rcpf(1.0f + __builtin_amdgcn_exp2f(g * -1.44269504f)); };
  auto epi2 = [&](const f32x16& ac, int i) { return pack2(act1(ac[2 * i], ac[8 + 2 * i]), act1(ac[2 * i + 1], ac[9 + 2 * i])); };

#define AT(x, q0, stmt) if constexpr (x == (q0)) { stmt; }
  const char* sta = wsA + (56 + 8 * j + 2 * nh + 3) * 1024;
  const char* sth = wsA + (2 * j + nh) * 1024;
  asm volatile("" : "+s"(sta), "+s"(sth));
  unsigned hw0, hw1;
  auto hook = [&](auto QI) {
    constexpr int q = QI;
    AT(q, 0, stage_w(5)) AT(q, 11, stage_w(6)) AT(q, 13, stage_w(7)) AT(q, 22, stage_w(8)) AT(q, 26, stage_w(9))
    AT(q, 28, stage_w(10)) AT(q, 33, stage_w(11)) AT(q, 36, poll()) AT(q, 38, stage_w(12)) AT(q, 44, stage_w(13))
    AT(q, 60, stage_p(7)) AT(q, 65, stage_p(8)) AT(q, 77, stage_p(0)) AT(q, 81, stage_p(1)) AT(q, 87, stage_p(6))
    AT(q, 93, stage_p(2)) AT(q, 97, stage_p(3)) AT(q, 108, stage_p(4)) AT(q, 109, stage_p(5))
    if constexpr (q >= 1 && q < 9) {
      constexpr int i = q - 1;
      auto sw = __builtin_amdgcn_permlane16_swap(__float_as_uint(accs[i]), __float_as_uint(accs[i + 8]), false, false);
      cmul(ur[i], ui[i], cfr, cfi, __uint_as_float(sw[0]), __uint_as_float(sw[1]));
      if constexpr (i == 0) { er = ur[0]; ei = ui[0]; } else cfma(er, ei, lr, li, er, ei, ur[i], ui[i]);
    }
    if constexpr (q == 9) { swap16(er, t0r, yr); swap16(ei, t0i, yi); cfma(er, ei, l8r, l8i, t0r, t0i, yr, yi); }
    if constexpr (q == 10) {
      swap32(er, t1r, yr); swap32(ei, t1i, yi);
      cfma(yr, yi, l16r, l16i, t1r, t1i, yr, yi);
      ldsw(segb + mq * 128, f32x2{yr, yi});
    }
    if constexpr (q == 12) {
#pragma unroll
      for (int i = 0; i < 4; ++i) sg[i] = ldsr<f32x2>(segb + i * 128);
      t1r = h ? t1r : 0.f; t1i = h ? t1i : 0.f; cfma(yr, yi, l8r, l8i, t1r, t1i, t0r, t0i);
    }
    if constexpr (q == 13) { clr = o16 ? yr : t1r; cli = o16 ? yi : t1i; pin2(clr, cli); }
    if constexpr (q == 14) cfma(t0r, t0i, l32r, l32i, sg[0][0], sg[0][1], sg[1][0], sg[1][1]);
    if constexpr (q == 15) cfma(t1r, t1i, l32r, l32i, t0r, t0i, sg[2][0], sg[2][1]);
    if constexpr (q == 16) cfma(pbr, pbi, l32r, l32i, t1r, t1i, sg[3][0], sg[3][1]);
    if constexpr (q == 17) publish();
    if constexpr (q == 18) {
      er = mq == 0 ? 0.f : (mq == 1 ? sg[0][0] : (mq == 2 ? t0r : t1r));
      ei = mq == 0 ? 0.f : (mq == 1 ? sg[0][1] : (mq == 2 ? t0i : t1i));
      pin2(er, ei);
    }
    if constexpr (q == 19) { yr = h ? l16r : 1.f; yi = h ? l16i : 0.f; t1r = o16 ? l8r : 1.f; t1i = o16 ? l8i : 0.f; pin2(yr, yi); pin2(t1r, t1i); }
    if constexpr (q == 20) cmul(yr, yi, yr, yi, t1r, t1i);
    if constexpr (q == 21) cfma(clr, cli, yr, yi, er, ei, clr, cli);
    if constexpr (q == 23) csq(l32r, l32i, l64r, l64i);
    if constexpr (q == 24) { t1r = (mq & 1) ? l32r : 1.f; t1i = (mq & 1) ? l32i : 0.f; cmul(yr, yi, yr, yi, t1r, t1i); }
    if constexpr (q == 25) { t0r = (mq & 2) ? l64r : 1.f; t0i = (mq & 2) ? l64i : 0.f; cmul(t0r, t0i, t0r, t0i, yr, yi); }
    if constexpr (q == 30) csq(l64r, l64i, lcr, lci);
    if constexpr (q == 31) csq(lcr, lci, l2r, l2i);
    if constexpr (q == 32) csq(l2r, l2i, l4r, l4i);
    if constexpr (q == 45) {
      wait4<4>(gr);
      for (int spin = 0; spin < SPIN; ++spin) {
        unsigned ok = (gr[0][1] & gr[0][3] & gr[1][1]) & (gr[1][3] & gr[2][1] & gr[2][3]) & (gr[3][1] & gr[3][3]);
        if (__all(ok != 0u)) break;
        __builtin_amdgcn_s_sleep(1);
        poll();
        wait4<0>(gr);
      }
      TS(29);
    }
    if constexpr (q == 46) { keepv(pbr); publish(); }
    if constexpr (q >= 47 && q < 51) {
      constexpr int n = 50 - q;
      bool v = 4 * n + dist < c;
      float xr = v ? __uint_as_float(gr[n][0]) : 0.f, xi = v ? __uint_as_float(gr[n][2]) : 0.f;
      if constexpr (n == 3) { yr = xr; yi = xi; pin2(yr, yi); }
      else cfma(yr, yi, l4r, l4i, yr, yi, xr, xi);
    }
    if constexpr (q == 51) { swap16(yr, er, t1r); swap16(yi, ei, t1i); pin2(er, t1r); pin2(ei, t1i); }
    if constexpr (q == 52) cfma(yr, yi, lcr, lci, t1r, t1i, er, ei);
    if constexpr (q == 53) { swap32(yr, er, t1r); swap32(yi, ei, t1i); pin2(er, t1r); pin2(ei, t1i); }
    if constexpr (q == 54) { cfma(yr, yi, l2r, l2i, t1r, t1i, er, ei); cfma(er, ei, t0r, t0i, yr, yi, clr, cli); }
    if constexpr (q == 55) {
      hw0 = lb + 6 * SLOT + w * 2048 + (16 * o16 + 4 * h) * 64 + ((c16 * 4) ^ (h << 4));
      hw1 = (hw0 ^ 32) + 512;
      keepu(hw0); keepu(hw1);
    }
    if constexpr (q >= 56 && q < 64) {
      constexpr int i = q - 56;
      cfma(er, ei, lr, li, er, ei, ur[i], ui[i]);
      ldsw((i < 4 ? hw0 : hw1) + (i & 3) * 64, pack2(er, ei));
    }
    if constexpr (q == 64) {
      cbar();
#pragma unroll
      for (int e = 0; e < 2; ++e) ov[e] = ldsr<u32x4>(lb + 6 * SLOT + w * 2048 + r * 64 + (((2 * h + e) ^ ((r >> 2) & 3)) << 4));
    }
    if constexpr (q == 66 || q == 67) {
      u32x4 o;
#pragma unroll
      for (int i = 0; i < 4; ++i)
        o[i] = __builtin_amdgcn_perm(ov[i >> 1][2 * (i & 1) + 1], ov[i >> 1][2 * (i & 1)], q == 66 ? 0x05040100u : 0x07060302u);
      st16o<0>(lane * 16, sth + (q - 66) * 28 * 1024, o);
    }
    if constexpr (q >= 68 && q < 100 && (q & 1) == 0) {
      constexpr int e = (q - 68) >> 1, t = e >> 3, i = e & 7;
      if constexpr ((i & 1) == 0) { t1r = act1(accu[0][t][i], accu[0][t][8 + i]); keepv(t1r); }
      else { unsigned pk = pack2(t1r, act1(accu[0][t][i], accu[0][t][8 + i])); keepu(pk); ov[t][i >> 1] = pk; }
    }
    AT(q, 83, st16o<-3072>(lane * 16, sta, ov[0])) AT(q, 99, st16o<-2048>(lane * 16, sta, ov[1]))
  };

  bar_vm<1>();
  TS(3);
  static_for<4>(load_x);
  static_for<3>(load_s);

  static_for<84>([&](auto SI) {
    constexpr int s = SI;
    if constexpr (s == 3 || s == 6) { bar_lgkm(); TS(s == 3 ? 4 : 5); }
    if constexpr (s == 17) { bar_vm<6>(); useu(warm); TS(6); }
    if constexpr (s == 26) { bar_vm<6>(); TS(7); }
    if constexpr (s == 34) { bar_vm<6>(); TS(8); }
    if constexpr (s == 42) { bar_vm<9>(); TS(9); }
    if constexpr (s == 50) { bar_vm<15>(); TS(10); }
    if constexpr (s == 58) { bar_vm<1>(); TS(11); }
    if constexpr (s == 66 || s == 74 || s == 82) { bar_lgkm(); TS(26 + (s - 66) / 8); }
    if constexpr (s < 28) {
      if constexpr (s < 6) static_for<4>([&](auto I) { load_x(std::integral_constant<int, 4 * (s + 1) + I>{}); });
      if constexpr (s + 3 < 28) load_s(std::integral_constant<int, s + 3>{});
      if constexpr (s >= 26) { load_w(std::integral_constant<int, s + 2>{}, 0); load_w(std::integral_constant<int, s + 2>{}, 1); }
      __builtin_amdgcn_sched_barrier(0);
      accs = mfma(xf[kstep_of(s)], fs[s & 3], accs);
      __builtin_amdgcn_sched_barrier(0);
      AT(s, 3, glds1<0>(vq, bb2 + 512, lw1); glds1<8192>(vq, bb2 + 640, lw1))
      AT(s, 6, stage_w(0)) AT(s, 10, stage_w(1))
      AT(s, 13, clear_inbox())
      AT(s, 17, stage_w(2)) AT(s, 21, stage_w(3)) AT(s, 26, stage_w(4))
      if constexpr (s == 16) {
        f32x4 pv = ldsr<f32x4>(lb + L_PAR + (16 * nh + c16) * 16);
        lr = pv[0]; li = pv[1]; cfr = pv[2]; cfi = pv[3];
      }
      if constexpr (s == 18) csq(lr, li, t0r, t0i);
      if constexpr (s == 20) csq(t0r, t0i, t1r, t1i);
      if constexpr (s == 22) csq(t1r, t1i, l8r, l8i);
      if constexpr (s == 23) csq(l8r, l8i, l16r, l16i);
      if constexpr (s == 25) csq(l16r, l16i, l32r, l32i);
      __builtin_amdgcn_sched_barrier(0);
    } else {
      constexpr int u = (s - 28) / 28;
      accu[u][0] = mfma(fr[s % 3][0], xf[kstep_of(s)], accu[u][0]);
      __builtin_amdgcn_sched_barrier(0);
      load_w(std::integral_constant<int, s + 2>{}, 0);
      hook(std::integral_constant<int, 2 * (s - 28)>{});
      __builtin_amdgcn_sched_barrier(0);
      accu[u][1] = mfma(fr[s % 3][1], xf[kstep_of(s)], accu[u][1]);
      __builtin_amdgcn_sched_barrier(0);
      load_w(std::integral_constant<int, s + 2>{}, 1);
      hook(std::integral_constant<int, 2 * (s - 28) + 1>{});
      __builtin_amdgcn_sched_barrier(0);
    }
  });
  TS(15);
#pragma unroll
  for (int t = 0; t < 2; ++t) {
#pragma unroll
    for (int i = 0; i < 4; ++i) ov[t][i] = epi2(accu[1][t], i);
    if (t == 0) st16o<1024>(lane * 16, sta, ov[t]); else st16o<2048>(lane * 16, sta, ov[t]);
  }
  }
  TS(16);
  bar_vm<0>();
  TS(17);

  int tid = threadIdx.x;
  asm volatile("" : "+v"(tid));
  const int c4 = tid & 15;
  u32x2 xv[2];
  const u32x2 dk = *(const u32x2*)(a.dsk + 64 * nt + 4 * c4);
  int trow[2];
#pragma unroll
  for (int e = 0; e < 2; ++e) {
    trow[e] = 128 * c + 64 * jg + 32 * e + tau(tid >> 4);
    xv[e] = *(const u32x2*)(a.x + (size_t)trow[e] * DM + 64 * nt + 4 * c4);
  }
  {
    const int wk = w < 4 ? w : 4 + (w & 1), mh = w < 4 ? 0 : (w - 4) >> 1;
    const int lane = tid & 63, r = lane & 31, h = lane >> 5, o16 = (lane >> 4) & 1, q4 = (lane >> 2) & 3;
    const int ln = min(lane, 13);
    GLB1 unsigned* const fo = (GLB1 unsigned*)(a.ws + OFF_FLG + (c * 14 + ln) * 128 + j * 4);
    GLB1 unsigned* const fi = (GLB1 unsigned*)(a.ws + OFF_FLG + (c * 14 + j) * 128 + ln * 4);
    if (w == 6) __hip_atomic_store(fo, 1u, __ATOMIC_RELAXED, __HIP_MEMORY_SCOPE_AGENT);
    unsigned toff[4];
#pragma unroll
    for (int ks = 0; ks < 4; ++ks) toff[ks] = lb + r * 128 + (((2 * ks + h) ^ ((r >> 1) & 7)) << 4);
    bf16x8 bq[3][4][2];
    const unsigned rg = wk * 24576;
    unsigned tra[2][2];
#pragma unroll
    for (int n = 0; n < 2; ++n)
#pragma unroll
      for (int e = 0; e < 2; ++e)
        tra[n][e] = lb + rg + (4 * h + q4 + 8 * e) * 128 + (((4 * n + 2 * o16 + ((lane >> 1) & 1)) ^ ((2 * h + (q4 >> 1) + 4 * e) & 7)) << 4) + (lane & 1) * 8;
    auto read_tr = [&](int u) {
#pragma unroll
      for (int i = 0; i < 4; ++i)
#pragma unroll
        for (int n = 0; n < 2; ++n) bq[u][i][n] = lds_tr(tra[n][0] + u * 8192 + i * 2048, tra[n][1] + u * 8192 + i * 2048);
    };
    auto read_quad = [&](int u) {
      if (w < 2) {
#pragma unroll
        for (int i = 0; i < 4; ++i)
#pragma unroll
          for (int n = 0; n < 2; ++n) bq[u][i][n] = lds_b128(toff[i] + rg + u * 8192 + n * 4096);
      } else read_tr(u);
    };
    const unsigned vq0 = (lane >> 3) * 896 + (((lane & 7) ^ (lane >> 4)) << 4);
    const unsigned vq1 = ((lane >> 3) + 8) * 896 + (((lane & 7) ^ (4 + (lane >> 4))) << 4) - 1024;
    const char* const qsrc = w >= 2 ? (const char*)a.wd + (size_t)(448 * (wk - 2) + 32 * mh) * 896 + nt * 128
                                    : (const char*)(w ? a.cim : a.cre) + (64 * nt) * 896;
    const int qstr = w >= 2 ? 64 * 896 : 128;
    const unsigned qdst = lb + rg + mh * 4096;
    const char* const pa = a.ws + OFF_A + (size_t)((4 * c + 2 * jg + mh) * KS_ALL + 28 * wk) * 1024;
    const unsigned l0 = lane * 16, l1 = lane * 16 + KS_ALL * 1024;
    const unsigned pw = lb + rg + (r + 32 * mh) * 272 + h * 16;
    auto piece = [&](auto QD, auto I) { glds2<(QD % 3) * 8192 + I * 2048>(vq0, vq1, qsrc + QD * qstr + I * (16 * 896), qdst); };
    auto put = [&](const f32x16& t, unsigned at) {
#pragma unroll
      for (int g = 0; g < 4; ++g) { f32x4 v = {t[4 * g], t[4 * g + 1], t[4 * g + 2], t[4 * g + 3]}; ldsw(at + g * 32, v); }
    };
    auto sync = [&](auto SI) {
      constexpr int s = SI;
      if constexpr (s == 8 || s == 23) wait_lgkm0();
      if constexpr (s == 3 || s == 8 || s == 19 || s == 23) asm volatile("s_barrier" ::: "memory");
    };
    if (w < 4) {
      u32x4 af[PA][2];
      f32x16 acc[2][2] = {};
      static_for<3>([&](auto U) {
        read_quad(U);
        wait_lgkm0();
        __builtin_amdgcn_sched_barrier(0);
        if constexpr (U == F1B) asm volatile("s_barrier" ::: "memory");
        static_for<4>([&](auto I) { piece(IC(3 + U), I); });
        __builtin_amdgcn_sched_barrier(0);
      });
      TS(20);
      asm volatile("s_barrier" ::: "memory");
      TS(21);
      auto lda = [&](int s) { gld16_sc1(af[s % PA][0], l0, pa + s * 1024); gld16_sc1(af[s % PA][1], l1, pa + s * 1024); };
#pragma unroll
      for (int s = 0; s < PA; ++s) lda(s);
      static_for<28>([&](auto SI) {
        constexpr int s = SI;
        wait2<young2(s, PA, 2, 12)>(af[s % PA][0], af[s % PA][1]);
#pragma unroll
        for (int m = 0; m < 2; ++m)
#pragma unroll
          for (int n = 0; n < 2; ++n)
            acc[m][n] = mfma(bq[(s >> 2) % 3][s & 3][n], __builtin_bit_cast(bf16x8, af[s % PA][m]), acc[m][n]);
        __builtin_amdgcn_sched_barrier(0);
        if constexpr (s + PA < 28) lda(s + PA);
        sync(SI);
        if constexpr (s >= 8 && s < 12) piece(IC(6), IC(s & 3));
        if constexpr (s == 3) read_quad(0);
        if constexpr (s == 11 || s == 15 || s == 19) read_quad(((s - 7) >> 2) % 3);
        if constexpr (s == 0) { TS(22); }
        __builtin_amdgcn_sched_barrier(0);
      });
      TS(23);
#pragma unroll
      for (int m = 0; m < 2; ++m)
#pragma unroll
        for (int n = 0; n < 2; ++n) put(acc[m][n], pw + m * 32 * 272 + n * 128);
    } else {
      u32x4 af[PH];
      f32x16 acc[2] = {};
      __builtin_amdgcn_s_setprio(2);
      read_tr(0); read_tr(1); read_tr(2);
      bar_lgkm();
      __builtin_amdgcn_sched_barrier(0);
      if (w != 6) static_for<3>([&](auto U) { piece(IC(3 + U), IC(0)); piece(IC(3 + U), IC(1)); });
      if (w == 4) static_for<3>([&](auto U) { piece(IC(3 + U), IC(2)); piece(IC(3 + U), IC(3)); });
      __builtin_amdgcn_sched_barrier(0);
      if (w == 6) {
        for (int spin = 0; spin < SPIN; ++spin) {
          unsigned v = __hip_atomic_load(fi, __ATOMIC_RELAXED, __HIP_MEMORY_SCOPE_AGENT);
          if (__all(v == 1u)) break;
          __builtin_amdgcn_s_sleep(1);
        }
      }
      TS(20);
      asm volatile("s_barrier" ::: "memory");
      TS(21);
      if (w == 6) __hip_atomic_store(fo, 1u, __ATOMIC_RELAXED, __HIP_MEMORY_SCOPE_AGENT);
      __builtin_amdgcn_sched_barrier(0);
#pragma unroll
      for (int s = 0; s < PH; ++s) gld16_sc1(af[s], l0, pa + s * 1024);
      static_for<28>([&](auto SI) {
        constexpr int s = SI;
        wait1<young2(s, PH, 1, 10)>(af[s % PH]);
#pragma unroll
        for (int n = 0; n < 2; ++n) acc[n] = mfma(bq[(s >> 2) % 3][s & 3][n], __builtin_bit_cast(bf16x8, af[s % PH]), acc[n]);
        __builtin_amdgcn_sched_barrier(0);
        if constexpr (s + PH < 28) gld16_sc1(af[s % PH], l0, pa + (s + PH) * 1024);
        sync(SI);
        if constexpr (s >= 8 && s < 10) piece(IC(6), IC(s & 1));
        if constexpr (s == 3) read_tr(0);
        if constexpr (s == 11 || s == 15 || s == 19) read_tr(((s - 7) >> 2) % 3);
        if constexpr (s == 0) { TS(22); }
        __builtin_amdgcn_sched_barrier(0);
      });
      TS(23);
      put(acc[0], pw); put(acc[1], pw + 128);
    }
  }
  bar_lgkm();
  TS(24);
#pragma unroll
  for (int e = 0; e < 2; ++e) {
    const int row = (tid >> 4) + 32 * e;
    const unsigned pr = lb + row * 272 + c4 * 16;
    f32x4 sv = ldsr<f32x4>(pr) - ldsr<f32x4>(pr + 24576);
    f32x4 dv = (ldsr<f32x4>(pr + 2 * 24576) + ldsr<f32x4>(pr + 3 * 24576)) + (ldsr<f32x4>(pr + 4 * 24576) + ldsr<f32x4>(pr + 5 * 24576));
    const f32x2 st = ldsr<f32x2>(lb + L_RS + (64 * jg + row) * 8);
    f32x4 o;
#pragma unroll
    for (int i = 0; i < 4; ++i) {
      unsigned xw = xv[e][i >> 1], dw = dk[i >> 1];
      float xf = __uint_as_float((i & 1) ? (xw & 0xffff0000u) : (xw << 16));
      float df = __uint_as_float((i & 1) ? (dw & 0xffff0000u) : (dw << 16));
      o[i] = xf + (2.0f * sv[i] + fmaf(xf, st[0], st[1]) * df) * silu(dv[i]);
    }
    st16((trow[e] * DM + 64 * nt + 4 * c4) * 4, a.out, __builtin_bit_cast(u32x4, o));
  }
}

extern "C" void kernel_launch(void* const* d_in, const int* in_sizes, int n_in, void* d_out, int out_size,
                              void* d_ws, size_t ws_size, hipStream_t stream) {
  Args a;
  a.x = (const unsigned short*)d_in[0];
  a.lre = (const unsigned short*)d_in[1];
  a.lim = (const unsigned short*)d_in[2];
  a.bre = (const unsigned short*)d_in[3];
  a.bim = (const unsigned short*)d_in[4];
  a.cre = (const unsigned short*)d_in[5];
  a.cim = (const unsigned short*)d_in[6];
  a.ldt = (const unsigned short*)d_in[7];
  a.dsk = (const unsigned short*)d_in[8];
  a.wg = (const unsigned short*)d_in[10];
  a.wu = (const unsigned short*)d_in[11];
  a.wd = (const unsigned short*)d_in[12];
  a.ws = (char*)d_ws;
  a.out = (float*)d_out;
  fused_kernel<<<256, 512, 0, stream>>>(a);
}
```
